```python
import torch

def _softmax(x, axis=-1):
    return torch.softmax(x, dim=axis)
def _gelu(x):
    return torch.nn.functional.gelu(x, approximate="tanh")
def _silu(x):
    return torch.nn.functional.silu(x)
def _sigmoid(x):
    return torch.sigmoid(x)
def _layernorm(x, axis=-1, eps=1e-5):
    m = x.mean(dim=axis, keepdim=True)
    v = x.var(dim=axis, keepdim=True, unbiased=False)
    return (x - m) / torch.sqrt(v + eps)
def _scatter_add(tgt, idx, src, axis=0):
    return tgt.clone().index_add_(axis, idx, src)
def _segment_sum(src, seg, n):
    out = torch.zeros((n,) + src.shape[1:], dtype=src.dtype)
    return out.index_add_(0, seg, src)
def _pad(x, pw):
    flat = tuple(v for pair in reversed(pw) for v in pair)
    return torch.nn.functional.pad(x, flat)

kernel_name = 'concat_heads__gqa_block_cached__gqa_block_cached__near_ridge'

def setup_inputs():

    _w = (lambda a: (lambda t: t.to(torch.bfloat16) if t.is_floating_point() else t)(torch.as_tensor(a)))
    def _A_setup():
        B, Sq, D = 16, 128, 576
        Hq, Hkv, Sk, Dh = 32, 16, 1024, 128
        assert Hq % Hkv == 0, (Hq, Hkv)
        return {
            "x": _w((lambda *s: torch.randn(*s, dtype=torch.bfloat16) * 0.8)(B, Sq, D)),
            "k_cache": _w((lambda *s: torch.randn(*s, dtype=torch.bfloat16) * 0.8)(B, Hkv, Sk, Dh)),
            "v_cache": _w((lambda *s: torch.randn(*s, dtype=torch.bfloat16) * 0.05)(B, Hkv, Sk, Dh)),
            "wq": _w((lambda *s: torch.randn(*s, dtype=torch.bfloat16) * 0.8)(D, Hq * Dh) * (D ** -0.5)),
            "wo": _w((lambda *s: torch.randn(*s, dtype=torch.bfloat16) * 0.05)(Hq * Dh, D) * ((Hq * Dh) ** -0.5)),
        }
    _A_in = _A_setup()

    def _B_setup():
        B, Sq, D = 16, 128, 576
        Hq, Hkv, Sk, Dh = 32, 16, 1024, 128
        assert Hq % Hkv == 0, (Hq, Hkv)
        return {
            "x": _w((lambda *s: torch.randn(*s, dtype=torch.bfloat16) * 0.8)(B, Sq, D)),
            "k_cache": _w((lambda *s: torch.randn(*s, dtype=torch.bfloat16) * 0.8)(B, Hkv, Sk, Dh)),
            "v_cache": _w((lambda *s: torch.randn(*s, dtype=torch.bfloat16) * 0.05)(B, Hkv, Sk, Dh)),
            "wq": _w((lambda *s: torch.randn(*s, dtype=torch.bfloat16) * 0.8)(D, Hq * Dh) * (D ** -0.5)),
            "wo": _w((lambda *s: torch.randn(*s, dtype=torch.bfloat16) * 0.05)(Hq * Dh, D) * ((Hq * Dh) ** -0.5)),
        }
    _B_in = _B_setup()

    return {
        "A_x": _A_in["x"],
        "A_k_cache": _A_in["k_cache"],
        "A_v_cache": _A_in["v_cache"],
        "A_wq": _A_in["wq"],
        "A_wo": _A_in["wo"],
        "B_x": _B_in["x"],
        "B_k_cache": _B_in["k_cache"],
        "B_v_cache": _B_in["v_cache"],
        "B_wq": _B_in["wq"],
        "B_wo": _B_in["wo"],
    }

def reference(A_x, A_k_cache, A_v_cache, A_wq, A_wo, B_x, B_k_cache, B_v_cache, B_wq, B_wo):
    def _A_ref(x, k_cache, v_cache, wq, wo):
        B, Sq, D = x.shape
        Hq, Dh = 32, 128
        Hkv = k_cache.shape[1]
        rep = Hq // Hkv
        q = (lambda x, *d: x.permute(*d))((x @ wq).reshape(B, Sq, Hq, Dh), 0, 2, 1, 3)
        k = (lambda x, n, axis: torch.repeat_interleave(x, n, dim=axis))(k_cache, rep, axis=1)
        v = (lambda x, n, axis: torch.repeat_interleave(x, n, dim=axis))(v_cache, rep, axis=1)
        scores = torch.einsum("bhqd,bhkd->bhqk", q, k) * (Dh ** -0.5)
        p = _softmax(scores, axis=-1)
        o = torch.einsum("bhqk,bhkd->bhqd", p, v)
        return (lambda x, *d: x.permute(*d))(o, 0, 2, 1, 3).reshape(B, Sq, Hq * Dh) @ wo

    def _B_ref(x, k_cache, v_cache, wq, wo):
        B, Sq, D = x.shape
        Hq, Dh = 32, 128
        Hkv = k_cache.shape[1]
        rep = Hq // Hkv
        q = (lambda x, *d: x.permute(*d))((x @ wq).reshape(B, Sq, Hq, Dh), 0, 2, 1, 3)
        k = (lambda x, n, axis: torch.repeat_interleave(x, n, dim=axis))(k_cache, rep, axis=1)
        v = (lambda x, n, axis: torch.repeat_interleave(x, n, dim=axis))(v_cache, rep, axis=1)
        scores = torch.einsum("bhqd,bhkd->bhqk", q, k) * (Dh ** -0.5)
        p = _softmax(scores, axis=-1)
        o = torch.einsum("bhqk,bhkd->bhqd", p, v)
        return (lambda x, *d: x.permute(*d))(o, 0, 2, 1, 3).reshape(B, Sq, Hq * Dh) @ wo

    a = _A_ref(A_x, A_k_cache, A_v_cache, A_wq, A_wo)
    b = _B_ref(B_x, B_k_cache, B_v_cache, B_wq, B_wo)
    return torch.concatenate([a, b], axis=-1)
```

```cpp
#include <hip/hip_runtime.h>
#include <hip/hip_bf16.h>
#include <cstdio>
#include <utility>

constexpr int D = 128, NWAVE = 4, QROWS = 64, KVBLK = 64;
constexpr float THR = 8.0f;
constexpr int NB = 16, SQ = 128, DM = 576, HQ = 32, HKV = 16, SK = 1024, NT = SK / KVBLK;
constexpr unsigned KRS = 256u, KTILEB = 64u * KRS;
constexpr size_t SHM_ATTN = 163840;
#define KVNT " nt"
typedef float    f32x16 __attribute__((ext_vector_type(16)));
typedef float    f32x2  __attribute__((ext_vector_type(2)));
typedef float    f32x4  __attribute__((ext_vector_type(4)));
typedef __bf16   bf16x8 __attribute__((ext_vector_type(8)));
typedef __bf16   bf16x4 __attribute__((ext_vector_type(4)));
typedef __bf16   bf16x2 __attribute__((ext_vector_type(2)));
typedef unsigned u32x4  __attribute__((ext_vector_type(4)));
typedef unsigned u32x2  __attribute__((ext_vector_type(2)));
template <int... Is> using iseq = std::integer_sequence<int, Is...>;
template <int N> using make_iseq = std::make_integer_sequence<int, N>;

#define KRING 0x0000u
#define VRING 0x8000u
#define QSTAGE 0x10000u
#define OSTAGE 0x20000u
#define SRD_WORD3 0x00020000
#define SLOT(t) ((((unsigned)(t)) & 1u) * 0x4000u + ((((unsigned)(t)) >> 1) & 1u) * 0x10000u)
#define DI __device__ __forceinline__
#define GAP() __builtin_amdgcn_sched_barrier(0)
#define W4_ACLOB_HI "a128","a129","a130","a131","a132","a133","a134","a135","a136","a137","a138","a139","a140","a141","a142","a143","a144","a145","a146","a147","a148","a149","a150","a151","a152","a153","a154","a155","a156","a157","a158","a159","a160","a161","a162","a163","a164","a165","a166","a167","a168","a169","a170","a171","a172","a173","a174","a175","a176","a177","a178","a179","a180","a181","a182","a183","a184","a185","a186","a187","a188","a189","a190","a191","a192","a193","a194","a195","a196","a197","a198","a199","a200","a201","a202","a203","a204","a205","a206","a207","a208","a209","a210","a211","a212","a213","a214","a215","a216","a217","a218","a219","a220","a221","a222","a223","a224","a225","a226","a227","a228","a229","a230","a231","a232","a233","a234","a235","a236","a237","a238","a239","a240","a241","a242","a243","a244","a245","a246","a247","a248","a249","a250","a251","a252","a253","a254","a255"
#define W4_ACLOB "a0","a1","a2","a3","a4","a5","a6","a7","a8","a9","a10","a11","a12","a13","a14","a15","a16","a17","a18","a19","a20","a21","a22","a23","a24","a25","a26","a27","a28","a29","a30","a31","a32","a33","a34","a35","a36","a37","a38","a39","a40","a41","a42","a43","a44","a45","a46","a47","a48","a49","a50","a51","a52","a53","a54","a55","a56","a57","a58","a59","a60","a61","a62","a63","a64","a65","a66","a67","a68","a69","a70","a71","a72","a73","a74","a75","a76","a77","a78","a79","a80","a81","a82","a83","a84","a85","a86","a87","a88","a89","a90","a91","a92","a93","a94","a95","a96","a97","a98","a99","a100","a101","a102","a103","a104","a105","a106","a107","a108","a109","a110","a111","a112","a113","a114","a115","a116","a117","a118","a119","a120","a121","a122","a123","a124","a125","a126","a127"
#define W4_READ_O(DB, QB, o) do { switch (2 * (DB) + (QB)) { \
    case 0: asm volatile("v_accvgpr_read_b32 %0, a0\n\t" "v_accvgpr_read_b32 %1, a1\n\t" "v_accvgpr_read_b32 %2, a2\n\t" "v_accvgpr_read_b32 %3, a3\n\t" "v_accvgpr_read_b32 %4, a4\n\t" "v_accvgpr_read_b32 %5, a5\n\t" "v_accvgpr_read_b32 %6, a6\n\t" "v_accvgpr_read_b32 %7, a7\n\t" "v_accvgpr_read_b32 %8, a8\n\t" "v_accvgpr_read_b32 %9, a9\n\t" "v_accvgpr_read_b32 %10, a10\n\t" "v_accvgpr_read_b32 %11, a11\n\t" "v_accvgpr_read_b32 %12, a12\n\t" "v_accvgpr_read_b32 %13, a13\n\t" "v_accvgpr_read_b32 %14, a14\n\t" "v_accvgpr_read_b32 %15, a15\n\t" : "=v"((o)[0]), "=v"((o)[1]), "=v"((o)[2]), "=v"((o)[3]), "=v"((o)[4]), "=v"((o)[5]), "=v"((o)[6]), "=v"((o)[7]), "=v"((o)[8]), "=v"((o)[9]), "=v"((o)[10]), "=v"((o)[11]), "=v"((o)[12]), "=v"((o)[13]), "=v"((o)[14]), "=v"((o)[15])); break; \
    case 1: asm volatile("v_accvgpr_read_b32 %0, a16\n\t" "v_accvgpr_read_b32 %1, a17\n\t" "v_accvgpr_read_b32 %2, a18\n\t" "v_accvgpr_read_b32 %3, a19\n\t" "v_accvgpr_read_b32 %4, a20\n\t" "v_accvgpr_read_b32 %5, a21\n\t" "v_accvgpr_read_b32 %6, a22\n\t" "v_accvgpr_read_b32 %7, a23\n\t" "v_accvgpr_read_b32 %8, a24\n\t" "v_accvgpr_read_b32 %9, a25\n\t" "v_accvgpr_read_b32 %10, a26\n\t" "v_accvgpr_read_b32 %11, a27\n\t" "v_accvgpr_read_b32 %12, a28\n\t" "v_accvgpr_read_b32 %13, a29\n\t" "v_accvgpr_read_b32 %14, a30\n\t" "v_accvgpr_read_b32 %15, a31\n\t" : "=v"((o)[0]), "=v"((o)[1]), "=v"((o)[2]), "=v"((o)[3]), "=v"((o)[4]), "=v"((o)[5]), "=v"((o)[6]), "=v"((o)[7]), "=v"((o)[8]), "=v"((o)[9]), "=v"((o)[10]), "=v"((o)[11]), "=v"((o)[12]), "=v"((o)[13]), "=v"((o)[14]), "=v"((o)[15])); break; \
    case 2: asm volatile("v_accvgpr_read_b32 %0, a32\n\t" "v_accvgpr_read_b32 %1, a33\n\t" "v_accvgpr_read_b32 %2, a34\n\t" "v_accvgpr_read_b32 %3, a35\n\t" "v_accvgpr_read_b32 %4, a36\n\t" "v_accvgpr_read_b32 %5, a37\n\t" "v_accvgpr_read_b32 %6, a38\n\t" "v_accvgpr_read_b32 %7, a39\n\t" "v_accvgpr_read_b32 %8, a40\n\t" "v_accvgpr_read_b32 %9, a41\n\t" "v_accvgpr_read_b32 %10, a42\n\t" "v_accvgpr_read_b32 %11, a43\n\t" "v_accvgpr_read_b32 %12, a44\n\t" "v_accvgpr_read_b32 %13, a45\n\t" "v_accvgpr_read_b32 %14, a46\n\t" "v_accvgpr_read_b32 %15, a47\n\t" : "=v"((o)[0]), "=v"((o)[1]), "=v"((o)[2]), "=v"((o)[3]), "=v"((o)[4]), "=v"((o)[5]), "=v"((o)[6]), "=v"((o)[7]), "=v"((o)[8]), "=v"((o)[9]), "=v"((o)[10]), "=v"((o)[11]), "=v"((o)[12]), "=v"((o)[13]), "=v"((o)[14]), "=v"((o)[15])); break; \
    case 3: asm volatile("v_accvgpr_read_b32 %0, a48\n\t" "v_accvgpr_read_b32 %1, a49\n\t" "v_accvgpr_read_b32 %2, a50\n\t" "v_accvgpr_read_b32 %3, a51\n\t" "v_accvgpr_read_b32 %4, a52\n\t" "v_accvgpr_read_b32 %5, a53\n\t" "v_accvgpr_read_b32 %6, a54\n\t" "v_accvgpr_read_b32 %7, a55\n\t" "v_accvgpr_read_b32 %8, a56\n\t" "v_accvgpr_read_b32 %9, a57\n\t" "v_accvgpr_read_b32 %10, a58\n\t" "v_accvgpr_read_b32 %11, a59\n\t" "v_accvgpr_read_b32 %12, a60\n\t" "v_accvgpr_read_b32 %13, a61\n\t" "v_accvgpr_read_b32 %14, a62\n\t" "v_accvgpr_read_b32 %15, a63\n\t" : "=v"((o)[0]), "=v"((o)[1]), "=v"((o)[2]), "=v"((o)[3]), "=v"((o)[4]), "=v"((o)[5]), "=v"((o)[6]), "=v"((o)[7]), "=v"((o)[8]), "=v"((o)[9]), "=v"((o)[10]), "=v"((o)[11]), "=v"((o)[12]), "=v"((o)[13]), "=v"((o)[14]), "=v"((o)[15])); break; \
    case 4: asm volatile("v_accvgpr_read_b32 %0, a64\n\t" "v_accvgpr_read_b32 %1, a65\n\t" "v_accvgpr_read_b32 %2, a66\n\t" "v_accvgpr_read_b32 %3, a67\n\t" "v_accvgpr_read_b32 %4, a68\n\t" "v_accvgpr_read_b32 %5, a69\n\t" "v_accvgpr_read_b32 %6, a70\n\t" "v_accvgpr_read_b32 %7, a71\n\t" "v_accvgpr_read_b32 %8, a72\n\t" "v_accvgpr_read_b32 %9, a73\n\t" "v_accvgpr_read_b32 %10, a74\n\t" "v_accvgpr_read_b32 %11, a75\n\t" "v_accvgpr_read_b32 %12, a76\n\t" "v_accvgpr_read_b32 %13, a77\n\t" "v_accvgpr_read_b32 %14, a78\n\t" "v_accvgpr_read_b32 %15, a79\n\t" : "=v"((o)[0]), "=v"((o)[1]), "=v"((o)[2]), "=v"((o)[3]), "=v"((o)[4]), "=v"((o)[5]), "=v"((o)[6]), "=v"((o)[7]), "=v"((o)[8]), "=v"((o)[9]), "=v"((o)[10]), "=v"((o)[11]), "=v"((o)[12]), "=v"((o)[13]), "=v"((o)[14]), "=v"((o)[15])); break; \
    case 5: asm volatile("v_accvgpr_read_b32 %0, a80\n\t" "v_accvgpr_read_b32 %1, a81\n\t" "v_accvgpr_read_b32 %2, a82\n\t" "v_accvgpr_read_b32 %3, a83\n\t" "v_accvgpr_read_b32 %4, a84\n\t" "v_accvgpr_read_b32 %5, a85\n\t" "v_accvgpr_read_b32 %6, a86\n\t" "v_accvgpr_read_b32 %7, a87\n\t" "v_accvgpr_read_b32 %8, a88\n\t" "v_accvgpr_read_b32 %9, a89\n\t" "v_accvgpr_read_b32 %10, a90\n\t" "v_accvgpr_read_b32 %11, a91\n\t" "v_accvgpr_read_b32 %12, a92\n\t" "v_accvgpr_read_b32 %13, a93\n\t" "v_accvgpr_read_b32 %14, a94\n\t" "v_accvgpr_read_b32 %15, a95\n\t" : "=v"((o)[0]), "=v"((o)[1]), "=v"((o)[2]), "=v"((o)[3]), "=v"((o)[4]), "=v"((o)[5]), "=v"((o)[6]), "=v"((o)[7]), "=v"((o)[8]), "=v"((o)[9]), "=v"((o)[10]), "=v"((o)[11]), "=v"((o)[12]), "=v"((o)[13]), "=v"((o)[14]), "=v"((o)[15])); break; \
    case 6: asm volatile("v_accvgpr_read_b32 %0, a96\n\t" "v_accvgpr_read_b32 %1, a97\n\t" "v_accvgpr_read_b32 %2, a98\n\t" "v_accvgpr_read_b32 %3, a99\n\t" "v_accvgpr_read_b32 %4, a100\n\t" "v_accvgpr_read_b32 %5, a101\n\t" "v_accvgpr_read_b32 %6, a102\n\t" "v_accvgpr_read_b32 %7, a103\n\t" "v_accvgpr_read_b32 %8, a104\n\t" "v_accvgpr_read_b32 %9, a105\n\t" "v_accvgpr_read_b32 %10, a106\n\t" "v_accvgpr_read_b32 %11, a107\n\t" "v_accvgpr_read_b32 %12, a108\n\t" "v_accvgpr_read_b32 %13, a109\n\t" "v_accvgpr_read_b32 %14, a110\n\t" "v_accvgpr_read_b32 %15, a111\n\t" : "=v"((o)[0]), "=v"((o)[1]), "=v"((o)[2]), "=v"((o)[3]), "=v"((o)[4]), "=v"((o)[5]), "=v"((o)[6]), "=v"((o)[7]), "=v"((o)[8]), "=v"((o)[9]), "=v"((o)[10]), "=v"((o)[11]), "=v"((o)[12]), "=v"((o)[13]), "=v"((o)[14]), "=v"((o)[15])); break; \
    case 7: asm volatile("v_accvgpr_read_b32 %0, a112\n\t" "v_accvgpr_read_b32 %1, a113\n\t" "v_accvgpr_read_b32 %2, a114\n\t" "v_accvgpr_read_b32 %3, a115\n\t" "v_accvgpr_read_b32 %4, a116\n\t" "v_accvgpr_read_b32 %5, a117\n\t" "v_accvgpr_read_b32 %6, a118\n\t" "v_accvgpr_read_b32 %7, a119\n\t" "v_accvgpr_read_b32 %8, a120\n\t" "v_accvgpr_read_b32 %9, a121\n\t" "v_accvgpr_read_b32 %10, a122\n\t" "v_accvgpr_read_b32 %11, a123\n\t" "v_accvgpr_read_b32 %12, a124\n\t" "v_accvgpr_read_b32 %13, a125\n\t" "v_accvgpr_read_b32 %14, a126\n\t" "v_accvgpr_read_b32 %15, a127\n\t" : "=v"((o)[0]), "=v"((o)[1]), "=v"((o)[2]), "=v"((o)[3]), "=v"((o)[4]), "=v"((o)[5]), "=v"((o)[6]), "=v"((o)[7]), "=v"((o)[8]), "=v"((o)[9]), "=v"((o)[10]), "=v"((o)[11]), "=v"((o)[12]), "=v"((o)[13]), "=v"((o)[14]), "=v"((o)[15])); break; \
    } } while (0)

DI unsigned vadd_vol(unsigned v, unsigned s) { unsigned r; asm volatile("v_add_u32_e32 %0, %1, %2" : "=v"(r) : "s"(s), "v"(v)); return r; }
DI unsigned rfl(unsigned x) { return __builtin_amdgcn_readfirstlane(x); }
DI unsigned cvtpk(float lo, float hi) {
  f32x2 v = {lo, hi}; bf16x2 b = __builtin_convertvector(v, bf16x2); return __builtin_bit_cast(unsigned, b);
}
DI float max3f(float a, float b, float c) { float r; asm("v_max3_f32 %0, %1, %2, %3" : "=v"(r) : "v"(a), "v"(b), "v"(c)); return r; }
DI float max2f(float a, float b) { float r; asm("v_max_f32_e32 %0, %1, %2" : "=v"(r) : "v"(a), "v"(b)); return r; }
DI float fadd_s(float a, float b) { float r; asm("v_add_f32_e32 %0, %1, %2" : "=v"(r) : "v"(a), "v"(b)); return r; }
DI float fmul_s(float a, float b) { float r; asm("v_mul_f32_e32 %0, %1, %2" : "=v"(r) : "v"(a), "v"(b)); return r; }
DI float fsub_s(float a, float b) { float r; asm("v_sub_f32_e32 %0, %1, %2" : "=v"(r) : "v"(a), "v"(b)); return r; }
DI float w4_aone() { unsigned l; asm volatile("v_mbcnt_lo_u32_b32 %0, -1, 0\n\tv_mbcnt_hi_u32_b32 %0, -1, %0" : "=v"(l)); return l < 32u ? 1.f : 0.f; }
DI void negm_bcast(f32x16& negm, float aone, float neg_mhat) {
  asm volatile("s_nop 1\n\tv_mfma_f32_32x32x2_f32 %0, %1, %2, 0" : "+v"(negm) : "v"(aone), "v"(neg_mhat));
}

struct W4C {
  unsigned kb4[4], vkb[4], vvb, vksrc[2], vvsrc, m0k, m0v, sofk, sofv, ldsb;
  __amdgpu_buffer_rsrc_t srdK, srdV;
  __amdgpu_buffer_rsrc_t srdKn, srdVn, srdQn; unsigned qsofn, qstepn, m0q;
  int T; float THR;
  f32x16 S[2][2][2];
  f32x16 NEGM[2];
  bf16x8 Vf[4][4];
  bf16x8 Pk[2][4];
  bf16x4 vlo[4][4], vup[4][4];
  float mhat[2], lsum[2], fct[2]; bool pend;
  float rs0[2], rs1[2], pmx0[2], pmx1[2], rmax[2], delta[2];
  unsigned pm0[8], psf[8];
};

constexpr int AO_(int db, int qb) { return 16 * (2 * db + qb); }
constexpr int AQ_(int qb, int d0) { return 128 + 32 * qb + 4 * d0; }
constexpr int AK_(int kb, int d0) { return 192 + 32 * kb + 4 * d0; }

template <int I> DI void tr_read(W4C& c, unsigned vslot) {
  constexpr int ks = I >> 3, db = (I >> 1) & 3;
  if constexpr ((I & 1) == 0) asm volatile("ds_read_b64_tr_b16 %0, %1 offset:%2" : "=v"(c.vlo[db][ks]) : "v"(vslot), "i"(4096 * ks + 512 * db) : "memory");
  else                        asm volatile("ds_read_b64_tr_b16 %0, %1 offset:%2" : "=v"(c.vup[db][ks]) : "v"(vslot), "i"(4096 * ks + 2048 + 512 * db) : "memory");
}
template <int I> DI void k_read(W4C& c, unsigned slot) {
  constexpr int kb = I >> 3, d0 = I & 7;
  asm volatile("ds_read_b128 a[%c1:%c2], %0 offset:%c3" :: "v"(c.kb4[d0 & 3]), "i"(AK_(kb, d0)), "i"(AK_(kb, d0) + 3), "i"(8192 * kb + 128 * (d0 >> 2)) : "memory", W4_ACLOB_HI);
}
template <int I> DI void q_write1(const u32x4& w) {
  constexpr int aq = AQ_(I >> 3, I & 7);
  asm volatile("v_accvgpr_write_b32 a[%c4], %0\n\tv_accvgpr_write_b32 a[%c5], %1\n\tv_accvgpr_write_b32 a[%c6], %2\n\tv_accvgpr_write_b32 a[%c7], %3"
               :: "v"(w[0]), "v"(w[1]), "v"(w[2]), "v"(w[3]), "i"(aq), "i"(aq + 1), "i"(aq + 2), "i"(aq + 3) : W4_ACLOB_HI);
}
DI void q_write(const int qb, const int d0, const u32x4& w) {
  switch (8 * qb + d0) {
    case 0: q_write1<0>(w); break;   case 1: q_write1<1>(w); break;   case 2: q_write1<2>(w); break;   case 3: q_write1<3>(w); break;
    case 4: q_write1<4>(w); break;   case 5: q_write1<5>(w); break;   case 6: q_write1<6>(w); break;   case 7: q_write1<7>(w); break;
    case 8: q_write1<8>(w); break;   case 9: q_write1<9>(w); break;   case 10: q_write1<10>(w); break; case 11: q_write1<11>(w); break;
    case 12: q_write1<12>(w); break; case 13: q_write1<13>(w); break; case 14: q_write1<14>(w); break; case 15: q_write1<15>(w); break;
  }
}
template <int X, bool FIRST, int M> DI void qk_mfma(W4C& c) {
  constexpr int d0 = M >> 2, kb = (M >> 1) & 1, qb = M & 1, ak = AK_(kb, d0), aq = AQ_(qb, d0);
  if constexpr (d0 == 0) {
    if constexpr (FIRST) asm volatile("v_mfma_f32_32x32x16_bf16 %0, a[%c1:%c2], a[%c3:%c4], 0" : "=&v"(c.S[X][kb][qb]) : "i"(ak), "i"(ak + 3), "i"(aq), "i"(aq + 3) : W4_ACLOB, W4_ACLOB_HI);
    else                 asm volatile("v_mfma_f32_32x32x16_bf16 %0, a[%c2:%c3], a[%c4:%c5], %1" : "=&v"(c.S[X][kb][qb]) : "v"(c.NEGM[qb]), "i"(ak), "i"(ak + 3), "i"(aq), "i"(aq + 3) : W4_ACLOB, W4_ACLOB_HI);
  } else asm volatile("v_mfma_f32_32x32x16_bf16 %0, a[%c1:%c2], a[%c3:%c4], %0" : "+v"(c.S[X][kb][qb]) : "i"(ak), "i"(ak + 3), "i"(aq), "i"(aq + 3) : W4_ACLOB, W4_ACLOB_HI);
}
template <int M, bool ZERO> DI void pv_mfma(W4C& c) {
  constexpr int ks = M >> 3, db = (M >> 1) & 3, qb = M & 1, ao = AO_(db, qb);
  if constexpr (ZERO && ks == 0) asm volatile("v_mfma_f32_32x32x16_bf16 a[%c2:%c3], %0, %1, 0" :: "v"(c.Vf[db][ks]), "v"(c.Pk[qb][ks]), "i"(ao), "i"(ao + 15) : W4_ACLOB);
  else                         asm volatile("v_mfma_f32_32x32x16_bf16 a[%c2:%c3], %0, %1, a[%c2:%c3]" :: "v"(c.Vf[db][ks]), "v"(c.Pk[qb][ks]), "i"(ao), "i"(ao + 15) : W4_ACLOB);
}
template <int X, int QB, int KS, int P> DI void pack1(W4C& c) {
  u32x4 w = __builtin_bit_cast(u32x4, c.Pk[QB][KS]);
  w[P] = cvtpk(c.S[X][KS >> 1][QB][8 * (KS & 1) + 2 * P], c.S[X][KS >> 1][QB][8 * (KS & 1) + 2 * P + 1]);
  c.Pk[QB][KS] = __builtin_bit_cast(bf16x8, w);
}
template <int X, int J> DI void rowsum1(W4C& c) {
  constexpr int qb = J >> 4, ks = (J >> 2) & 3, p = J & 3;
  c.rs0[qb] = fadd_s(c.rs0[qb], c.S[X][ks >> 1][qb][8 * (ks & 1) + 2 * p]); c.rs1[qb] = fadd_s(c.rs1[qb], c.S[X][ks >> 1][qb][8 * (ks & 1) + 2 * p + 1]);
}

template <int X, bool FIRST, int M, bool NEXTK = false, bool NEXTV = false> DI void gapA(W4C& c, unsigned vslot, const int t) {
  constexpr int Xp = X ^ 1;
  qk_mfma<X, FIRST, M>(c);
  if constexpr (M >= 16 && M <= 31) {
    const int tk = NEXTK ? t + 4 - c.T : t + 4, tv = NEXTV ? t + 3 - c.T : t + 3;
    constexpr int q = (M - 16) >> 1;
    if constexpr (((M - 16) & 1) == 0) {
      if constexpr (q < 4) c.pm0[q] = c.m0k + SLOT(t) + 1024u * q; else c.pm0[q] = c.m0v + SLOT(t + 3) + 1024u * (q - 4);
      asm volatile("" : "+s"(c.pm0[q]));
    } else {
      if constexpr (q < 4) c.psf[q] = KTILEB * (unsigned)tk + c.sofk + KRS * 4u * q; else c.psf[q] = KTILEB * (unsigned)tv + c.sofv + KRS * 8u * ((q - 4) >> 1) + 128u * ((q - 4) & 1);
      asm volatile("" : "+s"(c.psf[q]));
    }
  }
  if constexpr (M == 31) asm volatile("s_nop 4" : "+s"(c.pm0[0]), "+s"(c.pm0[1]), "+s"(c.pm0[2]), "+s"(c.pm0[3]), "+s"(c.pm0[4]), "+s"(c.pm0[5]), "+s"(c.pm0[6]), "+s"(c.pm0[7]),
                                                  "+s"(c.psf[0]), "+s"(c.psf[1]), "+s"(c.psf[2]), "+s"(c.psf[3]), "+s"(c.psf[4]), "+s"(c.psf[5]), "+s"(c.psf[6]), "+s"(c.psf[7]));
  if constexpr (!FIRST && M == 8) asm volatile("" :: "v"(c.NEGM[0]), "v"(c.NEGM[1]));
  if constexpr (!FIRST) {
    constexpr int base = M + (M >= 4) + (M >= 8) + (M >= 12) + (M >= 16) + (M >= 20) + (M >= 24);
    if constexpr (M <= 25) tr_read<base>(c, vslot);
    if constexpr (M == 3 || M == 7 || M == 11 || M == 15 || M == 19 || M == 23) tr_read<base + 1>(c, vslot);
    if constexpr (M < 16) {
      constexpr int e0 = 2 * M, e1 = 2 * M + 1;
      c.S[Xp][1][e0 >> 4][e0 & 15] = __builtin_amdgcn_exp2f(c.S[Xp][1][e0 >> 4][e0 & 15]);
      c.S[Xp][1][e1 >> 4][e1 & 15] = __builtin_amdgcn_exp2f(c.S[Xp][1][e1 >> 4][e1 & 15]);
      pack1<Xp, (M >> 3), ((M >> 2) & 1), (M & 3)>(c);
    } else {
      constexpr int cc = M - 16;
      pack1<Xp, (cc >> 3), 2 + ((cc >> 2) & 1), (cc & 3)>(c);
      rowsum1<Xp, 16 * (cc >> 3) + 8 + 4 * ((cc >> 2) & 1) + (cc & 3)>(c);
    }
  }
  GAP();
}
template <int X, bool FIRST, bool NEXTK, bool NEXTV, int... Ms> DI void phaseA_(W4C& c, const int t, iseq<Ms...>) {
  const unsigned vslot = vadd_vol(c.vvb, SLOT(t - 1));
  (gapA<X, FIRST, Ms, NEXTK, NEXTV>(c, vslot, t), ...);
  if constexpr (!FIRST) {
    #pragma unroll
    for (int qb = 0; qb < 2; ++qb) {
      const float rs = fadd_s(c.rs0[qb], c.rs1[qb]);
      auto pr = __builtin_amdgcn_permlane32_swap(__float_as_uint(rs), __float_as_uint(rs), false, false);
      c.lsum[qb] = fadd_s(c.lsum[qb], fadd_s(__uint_as_float(pr[0]), __uint_as_float(pr[1])));
    }
  }
#define W4_VLO "+v"(c.vlo[0][0]), "+v"(c.vlo[0][1]), "+v"(c.vlo[0][2]), "+v"(c.vlo[0][3]), "+v"(c.vlo[1][0]), "+v"(c.vlo[1][1]), "+v"(c.vlo[1][2]), "+v"(c.vlo[1][3]), \
               "+v"(c.vlo[2][0]), "+v"(c.vlo[2][1]), "+v"(c.vlo[2][2]), "+v"(c.vlo[2][3]), "+v"(c.vlo[3][0]), "+v"(c.vlo[3][1]), "+v"(c.vlo[3][2]), "+v"(c.vlo[3][3])
#define W4_VUP "+v"(c.vup[0][0]), "+v"(c.vup[0][1]), "+v"(c.vup[0][2]), "+v"(c.vup[0][3]), "+v"(c.vup[1][0]), "+v"(c.vup[1][1]), "+v"(c.vup[1][2]), "+v"(c.vup[1][3]), \
               "+v"(c.vup[2][0]), "+v"(c.vup[2][1]), "+v"(c.vup[2][2]), "+v"(c.vup[2][3]), "+v"(c.vup[3][0]), "+v"(c.vup[3][1]), "+v"(c.vup[3][2]), "+v"(c.vup[3][3])
  if constexpr (FIRST) asm volatile("s_waitcnt vmcnt(16) lgkmcnt(0)\n\ts_barrier" ::: "memory");
  else { asm volatile("s_waitcnt vmcnt(16) lgkmcnt(0)" : W4_VLO :: "memory"); asm volatile("s_barrier" : W4_VUP :: "memory"); }
  GAP();
  if constexpr (!FIRST) {
    #pragma unroll
    for (int ks = 0; ks < 4; ++ks)
      #pragma unroll
      for (int db = 0; db < 4; ++db) c.Vf[db][ks] = __builtin_shufflevector(c.vlo[db][ks], c.vup[db][ks], 0, 1, 2, 3, 4, 5, 6, 7);
  }
}
template <int X, bool FIRST, bool NEXTK = false, bool NEXTV = false> DI void phaseA(W4C& c, const int t) { phaseA_<X, FIRST, NEXTK, NEXTV>(c, t, make_iseq<32>{}); }

template <int X, bool PEEL, bool ZERO1, int DK, int DV, bool KR, int M> DI void gapB(W4C& c, const unsigned kslot_rd) {
  if constexpr (!PEEL) pv_mfma<M, ZERO1>(c);
  if constexpr (M < 4 && DK == 1)             asm volatile("s_mov_b32 m0, %0\n\ts_nop 0\n\tbuffer_load_dwordx4 %1, %2, %3 offen" KVNT " lds" :: "s"(c.pm0[M]), "v"(c.vksrc[M & 1]), "s"(c.srdK),  "s"(c.psf[M]) : "m0", "memory");
  else if constexpr (M < 4 && DK == 2)        asm volatile("s_mov_b32 m0, %0\n\ts_nop 0\n\tbuffer_load_dwordx4 %1, %2, %3 offen" KVNT " lds" :: "s"(c.pm0[M]), "v"(c.vksrc[M & 1]), "s"(c.srdKn), "s"(c.psf[M]) : "m0", "memory");
  else if constexpr (M >= 4 && M < 8 && DV == 1) asm volatile("s_mov_b32 m0, %0\n\ts_nop 0\n\tbuffer_load_dwordx4 %1, %2, %3 offen" KVNT " lds" :: "s"(c.pm0[M]), "v"(c.vvsrc), "s"(c.srdV),  "s"(c.psf[M]) : "m0", "memory");
  else if constexpr (M >= 4 && M < 8 && DV == 2) asm volatile("s_mov_b32 m0, %0\n\ts_nop 0\n\tbuffer_load_dwordx4 %1, %2, %3 offen" KVNT " lds" :: "s"(c.pm0[M]), "v"(c.vvsrc), "s"(c.srdVn), "s"(c.psf[M]) : "m0", "memory");
  if constexpr (M >= 1 && M <= 16 && KR) k_read<M - 1>(c, kslot_rd);
  if constexpr (M >= 8 && M < 16) {
    constexpr int i = M - 8, q = i >> 2, r = 4 * (i & 3);
    if constexpr ((i & 3) == 0) { c.pmx0[q] = max3f(c.S[X][0][q][0], c.S[X][0][q][1], c.S[X][1][q][0]); c.pmx1[q] = max3f(c.S[X][0][q][2], c.S[X][0][q][3], c.S[X][1][q][1]);
                                  c.pmx0[q] = max3f(c.pmx0[q], c.S[X][1][q][2], c.S[X][1][q][3]); }
    else                        { c.pmx0[q] = max3f(c.pmx0[q], c.S[X][0][q][r], c.S[X][0][q][r + 1]); c.pmx1[q] = max3f(c.pmx1[q], c.S[X][0][q][r + 2], c.S[X][0][q][r + 3]);
                                  c.pmx0[q] = max3f(c.pmx0[q], c.S[X][1][q][r], c.S[X][1][q][r + 1]); c.pmx1[q] = max3f(c.pmx1[q], c.S[X][1][q][r + 2], c.S[X][1][q][r + 3]); }
  }
  if constexpr (M == 16 || M == 17) {
    constexpr int q = M - 16; const float mm = max2f(c.pmx0[q], c.pmx1[q]);
    auto pr = __builtin_amdgcn_permlane32_swap(__float_as_uint(mm), __float_as_uint(mm), false, false);
    c.rmax[q] = max2f(__uint_as_float(pr[0]), __uint_as_float(pr[1]));
  }
  if constexpr (M == 18) {
    if constexpr (PEEL) {
      #pragma unroll
      for (int q = 0; q < 2; ++q) { c.mhat[q] = c.rmax[q]; c.lsum[q] = 0.f; c.fct[q] = 1.f;
        #pragma unroll
        for (int kb = 0; kb < 2; ++kb) { _Pragma("unroll") for (int e = 0; e < 16; ++e) c.S[X][kb][q][e] = fsub_s(c.S[X][kb][q][e], c.rmax[q]); }
        negm_bcast(c.NEGM[q], w4_aone(), -c.mhat[q]);
      }
    } else {
      const bool grew = (c.rmax[0] > c.THR) | (c.rmax[1] > c.THR);
      if (__builtin_expect(__any((int)grew), 0)) {
        #pragma unroll
        for (int q = 0; q < 2; ++q) { const float dl = max2f(c.rmax[q], 0.f); c.delta[q] = dl; c.mhat[q] = fadd_s(c.mhat[q], dl); c.fct[q] = __builtin_amdgcn_exp2f(-dl);
          #pragma unroll
          for (int kb = 0; kb < 2; ++kb) { _Pragma("unroll") for (int e = 0; e < 16; ++e) c.S[X][kb][q][e] = fsub_s(c.S[X][kb][q][e], dl); }
          negm_bcast(c.NEGM[q], w4_aone(), -c.mhat[q]);
        }
        c.pend = true;
      }
    }
  }
  if constexpr (M == 18) { c.rs0[0] = c.rs0[1] = c.rs1[0] = c.rs1[1] = 0.f; }
  if constexpr (M >= 19) {
    constexpr int g = M - 19, e_lo = (32 * g) / 13, e_hi = (32 * (g + 1)) / 13;
    {
      constexpr int p_lo = g ? (32 * (g - 1)) / 13 : 0, p_hi = g ? (32 * g) / 13 : 0;
      if constexpr (p_lo + 0 < p_hi) { constexpr int e = p_lo + 0; if constexpr (e & 1) c.rs1[e >> 4] = fadd_s(c.rs1[e >> 4], c.S[X][0][e >> 4][e & 15]); else c.rs0[e >> 4] = fadd_s(c.rs0[e >> 4], c.S[X][0][e >> 4][e & 15]); }
      if constexpr (p_lo + 1 < p_hi) { constexpr int e = p_lo + 1; if constexpr (e & 1) c.rs1[e >> 4] = fadd_s(c.rs1[e >> 4], c.S[X][0][e >> 4][e & 15]); else c.rs0[e >> 4] = fadd_s(c.rs0[e >> 4], c.S[X][0][e >> 4][e & 15]); }
      if constexpr (p_lo + 2 < p_hi) { constexpr int e = p_lo + 2; if constexpr (e & 1) c.rs1[e >> 4] = fadd_s(c.rs1[e >> 4], c.S[X][0][e >> 4][e & 15]); else c.rs0[e >> 4] = fadd_s(c.rs0[e >> 4], c.S[X][0][e >> 4][e & 15]); }
    }
    if constexpr (e_lo + 0 < e_hi) c.S[X][0][(e_lo + 0) >> 4][(e_lo + 0) & 15] = __builtin_amdgcn_exp2f(c.S[X][0][(e_lo + 0) >> 4][(e_lo + 0) & 15]);
    if constexpr (e_lo + 1 < e_hi) c.S[X][0][(e_lo + 1) >> 4][(e_lo + 1) & 15] = __builtin_amdgcn_exp2f(c.S[X][0][(e_lo + 1) >> 4][(e_lo + 1) & 15]);
    if constexpr (e_lo + 2 < e_hi) c.S[X][0][(e_lo + 2) >> 4][(e_lo + 2) & 15] = __builtin_amdgcn_exp2f(c.S[X][0][(e_lo + 2) >> 4][(e_lo + 2) & 15]);
  }
  if constexpr (M == 31) asm volatile("" :: "v"(c.S[X][0][0]), "v"(c.S[X][0][1]), "v"(c.NEGM[0]), "v"(c.NEGM[1]), "v"(c.rs0[0]), "v"(c.rs0[1]), "v"(c.rs1[0]), "v"(c.rs1[1]));
  GAP();
}
template <int X, bool PEEL, bool ZERO1, int DK, int DV, bool KR, int... Ms> DI void phaseB_(W4C& c, const int t, iseq<Ms...>) {
  const unsigned kslot_rd = SLOT(t + 1); if constexpr (KR) { _Pragma("unroll") for (int j = 0; j < 4; ++j) c.kb4[j] = vadd_vol(c.vkb[j], kslot_rd); }
  if constexpr (!PEEL) asm volatile("s_nop 1" ::: "memory");
  GAP();
  (gapB<X, PEEL, ZERO1, DK, DV, KR, Ms>(c, kslot_rd), ...);
  {
    #pragma unroll
    for (int e = (32 * 12) / 13; e < 32; ++e) { if (e & 1) c.rs1[e >> 4] = fadd_s(c.rs1[e >> 4], c.S[X][0][e >> 4][e & 15]); else c.rs0[e >> 4] = fadd_s(c.rs0[e >> 4], c.S[X][0][e >> 4][e & 15]); }
  }
  if constexpr (!PEEL) {
    if (__builtin_expect(c.pend, 0)) {
      float t0, t1;
      asm volatile("s_nop 7\n\ts_nop 7\n\t"
        "v_accvgpr_read_b32 %0, a0\n\tv_mul_f32 %0, %2, %0\n\tv_accvgpr_write_b32 a0, %0\n\t"
        "v_accvgpr_read_b32 %1, a1\n\tv_mul_f32 %1, %2, %1\n\tv_accvgpr_write_b32 a1, %1\n\t"
        "v_accvgpr_read_b32 %0, a2\n\tv_mul_f32 %0, %2, %0\n\tv_accvgpr_write_b32 a2, %0\n\t"
        "v_accvgpr_read_b32 %1, a3\n\tv_mul_f32 %1, %2, %1\n\tv_accvgpr_write_b32 a3, %1\n\t"
        "v_accvgpr_read_b32 %0, a4\n\tv_mul_f32 %0, %2, %0\n\tv_accvgpr_write_b32 a4, %0\n\t"
        "v_accvgpr_read_b32 %1, a5\n\tv_mul_f32 %1, %2, %1\n\tv_accvgpr_write_b32 a5, %1\n\t"
        "v_accvgpr_read_b32 %0, a6\n\tv_mul_f32 %0, %2, %0\n\tv_accvgpr_write_b32 a6, %0\n\t"
        "v_accvgpr_read_b32 %1, a7\n\tv_mul_f32 %1, %2, %1\n\tv_accvgpr_write_b32 a7, %1\n\t"
        "v_accvgpr_read_b32 %0, a8\n\tv_mul_f32 %0, %2, %0\n\tv_accvgpr_write_b32 a8, %0\n\t"
        "v_accvgpr_read_b32 %1, a9\n\tv_mul_f32 %1, %2, %1\n\tv_accvgpr_write_b32 a9, %1\n\t"
        "v_accvgpr_read_b32 %0, a10\n\tv_mul_f32 %0, %2, %0\n\tv_accvgpr_write_b32 a10, %0\n\t"
        "v_accvgpr_read_b32 %1, a11\n\tv_mul_f32 %1, %2, %1\n\tv_accvgpr_write_b32 a11, %1\n\t"
        "v_accvgpr_read_b32 %0, a12\n\tv_mul_f32 %0, %2, %0\n\tv_accvgpr_write_b32 a12, %0\n\t"
        "v_accvgpr_read_b32 %1, a13\n\tv_mul_f32 %1, %2, %1\n\tv_accvgpr_write_b32 a13, %1\n\t"
        "v_accvgpr_read_b32 %0, a14\n\tv_mul_f32 %0, %2, %0\n\tv_accvgpr_write_b32 a14, %0\n\t"
        "v_accvgpr_read_b32 %1, a15\n\tv_mul_f32 %1, %2, %1\n\tv_accvgpr_write_b32 a15, %1\n\t"
        "v_accvgpr_read_b32 %0, a16\n\tv_mul_f32 %0, %3, %0\n\tv_accvgpr_write_b32 a16, %0\n\t"
        "v_accvgpr_read_b32 %1, a17\n\tv_mul_f32 %1, %3, %1\n\tv_accvgpr_write_b32 a17, %1\n\t"
        "v_accvgpr_read_b32 %0, a18\n\tv_mul_f32 %0, %3, %0\n\tv_accvgpr_write_b32 a18, %0\n\t"
        "v_accvgpr_read_b32 %1, a19\n\tv_mul_f32 %1, %3, %1\n\tv_accvgpr_write_b32 a19, %1\n\t"
        "v_accvgpr_read_b32 %0, a20\n\tv_mul_f32 %0, %3, %0\n\tv_accvgpr_write_b32 a20, %0\n\t"
        "v_accvgpr_read_b32 %1, a21\n\tv_mul_f32 %1, %3, %1\n\tv_accvgpr_write_b32 a21, %1\n\t"
        "v_accvgpr_read_b32 %0, a22\n\tv_mul_f32 %0, %3, %0\n\tv_accvgpr_write_b32 a22, %0\n\t"
        "v_accvgpr_read_b32 %1, a23\n\tv_mul_f32 %1, %3, %1\n\tv_accvgpr_write_b32 a23, %1\n\t"
        "v_accvgpr_read_b32 %0, a24\n\tv_mul_f32 %0, %3, %0\n\tv_accvgpr_write_b32 a24, %0\n\t"
        "v_accvgpr_read_b32 %1, a25\n\tv_mul_f32 %1, %3, %1\n\tv_accvgpr_write_b32 a25, %1\n\t"
        "v_accvgpr_read_b32 %0, a26\n\tv_mul_f32 %0, %3, %0\n\tv_accvgpr_write_b32 a26, %0\n\t"
        "v_accvgpr_read_b32 %1, a27\n\tv_mul_f32 %1, %3, %1\n\tv_accvgpr_write_b32 a27, %1\n\t"
        "v_accvgpr_read_b32 %0, a28\n\tv_mul_f32 %0, %3, %0\n\tv_accvgpr_write_b32 a28, %0\n\t"
        "v_accvgpr_read_b32 %1, a29\n\tv_mul_f32 %1, %3, %1\n\tv_accvgpr_write_b32 a29, %1\n\t"
        "v_accvgpr_read_b32 %0, a30\n\tv_mul_f32 %0, %3, %0\n\tv_accvgpr_write_b32 a30, %0\n\t"
        "v_accvgpr_read_b32 %1, a31\n\tv_mul_f32 %1, %3, %1\n\tv_accvgpr_write_b32 a31, %1\n\t"
        "v_accvgpr_read_b32 %0, a32\n\tv_mul_f32 %0, %2, %0\n\tv_accvgpr_write_b32 a32, %0\n\t"
        "v_accvgpr_read_b32 %1, a33\n\tv_mul_f32 %1, %2, %1\n\tv_accvgpr_write_b32 a33, %1\n\t"
        "v_accvgpr_read_b32 %0, a34\n\tv_mul_f32 %0, %2, %0\n\tv_accvgpr_write_b32 a34, %0\n\t"
        "v_accvgpr_read_b32 %1, a35\n\tv_mul_f32 %1, %2, %1\n\tv_accvgpr_write_b32 a35, %1\n\t"
        "v_accvgpr_read_b32 %0, a36\n\tv_mul_f32 %0, %2, %0\n\tv_accvgpr_write_b32 a36, %0\n\t"
        "v_accvgpr_read_b32 %1, a37\n\tv_mul_f32 %1, %2, %1\n\tv_accvgpr_write_b32 a37, %1\n\t"
        "v_accvgpr_read_b32 %0, a38\n\tv_mul_f32 %0, %2, %0\n\tv_accvgpr_write_b32 a38, %0\n\t"
        "v_accvgpr_read_b32 %1, a39\n\tv_mul_f32 %1, %2, %1\n\tv_accvgpr_write_b32 a39, %1\n\t"
        "v_accvgpr_read_b32 %0, a40\n\tv_mul_f32 %0, %2, %0\n\tv_accvgpr_write_b32 a40, %0\n\t"
        "v_accvgpr_read_b32 %1, a41\n\tv_mul_f32 %1, %2, %1\n\tv_accvgpr_write_b32 a41, %1\n\t"
        "v_accvgpr_read_b32 %0, a42\n\tv_mul_f32 %0, %2, %0\n\tv_accvgpr_write_b32 a42, %0\n\t"
        "v_accvgpr_read_b32 %1, a43\n\tv_mul_f32 %1, %2, %1\n\tv_accvgpr_write_b32 a43, %1\n\t"
        "v_accvgpr_read_b32 %0, a44\n\tv_mul_f32 %0, %2, %0\n\tv_accvgpr_write_b32 a44, %0\n\t"
        "v_accvgpr_read_b32 %1, a45\n\tv_mul_f32 %1, %2, %1\n\tv_accvgpr_write_b32 a45, %1\n\t"
        "v_accvgpr_read_b32 %0, a46\n\tv_mul_f32 %0, %2, %0\n\tv_accvgpr_write_b32 a46, %0\n\t"
        "v_accvgpr_read_b32 %1, a47\n\tv_mul_f32 %1, %2, %1\n\tv_accvgpr_write_b32 a47, %1\n\t"
        "v_accvgpr_read_b32 %0, a48\n\tv_mul_f32 %0, %3, %0\n\tv_accvgpr_write_b32 a48, %0\n\t"
        "v_accvgpr_read_b32 %1, a49\n\tv_mul_f32 %1, %3, %1\n\tv_accvgpr_write_b32 a49, %1\n\t"
        "v_accvgpr_read_b32 %0, a50\n\tv_mul_f32 %0, %3, %0\n\tv_accvgpr_write_b32 a50, %0\n\t"
        "v_accvgpr_read_b32 %1, a51\n\tv_mul_f32 %1, %3, %1\n\tv_accvgpr_write_b32 a51, %1\n\t"
        "v_accvgpr_read_b32 %0, a52\n\tv_mul_f32 %0, %3, %0\n\tv_accvgpr_write_b32 a52, %0\n\t"
        "v_accvgpr_read_b32 %1, a53\n\tv_mul_f32 %1, %3, %1\n\tv_accvgpr_write_b32 a53, %1\n\t"
        "v_accvgpr_read_b32 %0, a54\n\tv_mul_f32 %0, %3, %0\n\tv_accvgpr_write_b32 a54, %0\n\t"
        "v_accvgpr_read_b32 %1, a55\n\tv_mul_f32 %1, %3, %1\n\tv_accvgpr_write_b32 a55, %1\n\t"
        "v_accvgpr_read_b32 %0, a56\n\tv_mul_f32 %0, %3, %0\n\tv_accvgpr_write_b32 a56, %0\n\t"
        "v_accvgpr_read_b32 %1, a57\n\tv_mul_f32 %1, %3, %1\n\tv_accvgpr_write_b32 a57, %1\n\t"
        "v_accvgpr_read_b32 %0, a58\n\tv_mul_f32 %0, %3, %0\n\tv_accvgpr_write_b32 a58, %0\n\t"
        "v_accvgpr_read_b32 %1, a59\n\tv_mul_f32 %1, %3, %1\n\tv_accvgpr_write_b32 a59, %1\n\t"
        "v_accvgpr_read_b32 %0, a60\n\tv_mul_f32 %0, %3, %0\n\tv_accvgpr_write_b32 a60, %0\n\t"
        "v_accvgpr_read_b32 %1, a61\n\tv_mul_f32 %1, %3, %1\n\tv_accvgpr_write_b32 a61, %1\n\t"
        "v_accvgpr_read_b32 %0, a62\n\tv_mul_f32 %0, %3, %0\n\tv_accvgpr_write_b32 a62, %0\n\t"
        "v_accvgpr_read_b32 %1, a63\n\tv_mul_f32 %1, %3, %1\n\tv_accvgpr_write_b32 a63, %1\n\t"
        "v_accvgpr_read_b32 %0, a64\n\tv_mul_f32 %0, %2, %0\n\tv_accvgpr_write_b32 a64, %0\n\t"
        "v_accvgpr_read_b32 %1, a65\n\tv_mul_f32 %1, %2, %1\n\tv_accvgpr_write_b32 a65, %1\n\t"
        "v_accvgpr_read_b32 %0, a66\n\tv_mul_f32 %0, %2, %0\n\tv_accvgpr_write_b32 a66, %0\n\t"
        "v_accvgpr_read_b32 %1, a67\n\tv_mul_f32 %1, %2, %1\n\tv_accvgpr_write_b32 a67, %1\n\t"
        "v_accvgpr_read_b32 %0, a68\n\tv_mul_f32 %0, %2, %0\n\tv_accvgpr_write_b32 a68, %0\n\t"
        "v_accvgpr_read_b32 %1, a69\n\tv_mul_f32 %1, %2, %1\n\tv_accvgpr_write_b32 a69, %1\n\t"
        "v_accvgpr_read_b32 %0, a70\n\tv_mul_f32 %0, %2, %0\n\tv_accvgpr_write_b32 a70, %0\n\t"
        "v_accvgpr_read_b32 %1, a71\n\tv_mul_f32 %1, %2, %1\n\tv_accvgpr_write_b32 a71, %1\n\t"
        "v_accvgpr_read_b32 %0, a72\n\tv_mul_f32 %0, %2, %0\n\tv_accvgpr_write_b32 a72, %0\n\t"
        "v_accvgpr_read_b32 %1, a73\n\tv_mul_f32 %1, %2, %1\n\tv_accvgpr_write_b32 a73, %1\n\t"
        "v_accvgpr_read_b32 %0, a74\n\tv_mul_f32 %0, %2, %0\n\tv_accvgpr_write_b32 a74, %0\n\t"
        "v_accvgpr_read_b32 %1, a75\n\tv_mul_f32 %1, %2, %1\n\tv_accvgpr_write_b32 a75, %1\n\t"
        "v_accvgpr_read_b32 %0, a76\n\tv_mul_f32 %0, %2, %0\n\tv_accvgpr_write_b32 a76, %0\n\t"
        "v_accvgpr_read_b32 %1, a77\n\tv_mul_f32 %1, %2, %1\n\tv_accvgpr_write_b32 a77, %1\n\t"
        "v_accvgpr_read_b32 %0, a78\n\tv_mul_f32 %0, %2, %0\n\tv_accvgpr_write_b32 a78, %0\n\t"
        "v_accvgpr_read_b32 %1, a79\n\tv_mul_f32 %1, %2, %1\n\tv_accvgpr_write_b32 a79, %1\n\t"
        "v_accvgpr_read_b32 %0, a80\n\tv_mul_f32 %0, %3, %0\n\tv_accvgpr_write_b32 a80, %0\n\t"
        "v_accvgpr_read_b32 %1, a81\n\tv_mul_f32 %1, %3, %1\n\tv_accvgpr_write_b32 a81, %1\n\t"
        "v_accvgpr_read_b32 %0, a82\n\tv_mul_f32 %0, %3, %0\n\tv_accvgpr_write_b32 a82, %0\n\t"
        "v_accvgpr_read_b32 %1, a83\n\tv_mul_f32 %1, %3, %1\n\tv_accvgpr_write_b32 a83, %1\n\t"
        "v_accvgpr_read_b32 %0, a84\n\tv_mul_f32 %0, %3, %0\n\tv_accvgpr_write_b32 a84, %0\n\t"
        "v_accvgpr_read_b32 %1, a85\n\tv_mul_f32 %1, %3, %1\n\tv_accvgpr_write_b32 a85, %1\n\t"
        "v_accvgpr_read_b32 %0, a86\n\tv_mul_f32 %0, %3, %0\n\tv_accvgpr_write_b32 a86, %0\n\t"
        "v_accvgpr_read_b32 %1, a87\n\tv_mul_f32 %1, %3, %1\n\tv_accvgpr_write_b32 a87, %1\n\t"
        "v_accvgpr_read_b32 %0, a88\n\tv_mul_f32 %0, %3, %0\n\tv_accvgpr_write_b32 a88, %0\n\t"
        "v_accvgpr_read_b32 %1, a89\n\tv_mul_f32 %1, %3, %1\n\tv_accvgpr_write_b32 a89, %1\n\t"
        "v_accvgpr_read_b32 %0, a90\n\tv_mul_f32 %0, %3, %0\n\tv_accvgpr_write_b32 a90, %0\n\t"
        "v_accvgpr_read_b32 %1, a91\n\tv_mul_f32 %1, %3, %1\n\tv_accvgpr_write_b32 a91, %1\n\t"
        "v_accvgpr_read_b32 %0, a92\n\tv_mul_f32 %0, %3, %0\n\tv_accvgpr_write_b32 a92, %0\n\t"
        "v_accvgpr_read_b32 %1, a93\n\tv_mul_f32 %1, %3, %1\n\tv_accvgpr_write_b32 a93, %1\n\t"
        "v_accvgpr_read_b32 %0, a94\n\tv_mul_f32 %0, %3, %0\n\tv_accvgpr_write_b32 a94, %0\n\t"
        "v_accvgpr_read_b32 %1, a95\n\tv_mul_f32 %1, %3, %1\n\tv_accvgpr_write_b32 a95, %1\n\t"
        "v_accvgpr_read_b32 %0, a96\n\tv_mul_f32 %0, %2, %0\n\tv_accvgpr_write_b32 a96, %0\n\t"
        "v_accvgpr_read_b32 %1, a97\n\tv_mul_f32 %1, %2, %1\n\tv_accvgpr_write_b32 a97, %1\n\t"
        "v_accvgpr_read_b32 %0, a98\n\tv_mul_f32 %0, %2, %0\n\tv_accvgpr_write_b32 a98, %0\n\t"
        "v_accvgpr_read_b32 %1, a99\n\tv_mul_f32 %1, %2, %1\n\tv_accvgpr_write_b32 a99, %1\n\t"
        "v_accvgpr_read_b32 %0, a100\n\tv_mul_f32 %0, %2, %0\n\tv_accvgpr_write_b32 a100, %0\n\t"
        "v_accvgpr_read_b32 %1, a101\n\tv_mul_f32 %1, %2, %1\n\tv_accvgpr_write_b32 a101, %1\n\t"
        "v_accvgpr_read_b32 %0, a102\n\tv_mul_f32 %0, %2, %0\n\tv_accvgpr_write_b32 a102, %0\n\t"
        "v_accvgpr_read_b32 %1, a103\n\tv_mul_f32 %1, %2, %1\n\tv_accvgpr_write_b32 a103, %1\n\t"
        "v_accvgpr_read_b32 %0, a104\n\tv_mul_f32 %0, %2, %0\n\tv_accvgpr_write_b32 a104, %0\n\t"
        "v_accvgpr_read_b32 %1, a105\n\tv_mul_f32 %1, %2, %1\n\tv_accvgpr_write_b32 a105, %1\n\t"
        "v_accvgpr_read_b32 %0, a106\n\tv_mul_f32 %0, %2, %0\n\tv_accvgpr_write_b32 a106, %0\n\t"
        "v_accvgpr_read_b32 %1, a107\n\tv_mul_f32 %1, %2, %1\n\tv_accvgpr_write_b32 a107, %1\n\t"
        "v_accvgpr_read_b32 %0, a108\n\tv_mul_f32 %0, %2, %0\n\tv_accvgpr_write_b32 a108, %0\n\t"
        "v_accvgpr_read_b32 %1, a109\n\tv_mul_f32 %1, %2, %1\n\tv_accvgpr_write_b32 a109, %1\n\t"
        "v_accvgpr_read_b32 %0, a110\n\tv_mul_f32 %0, %2, %0\n\tv_accvgpr_write_b32 a110, %0\n\t"
        "v_accvgpr_read_b32 %1, a111\n\tv_mul_f32 %1, %2, %1\n\tv_accvgpr_write_b32 a111, %1\n\t"
        "v_accvgpr_read_b32 %0, a112\n\tv_mul_f32 %0, %3, %0\n\tv_accvgpr_write_b32 a112, %0\n\t"
        "v_accvgpr_read_b32 %1, a113\n\tv_mul_f32 %1, %3, %1\n\tv_accvgpr_write_b32 a113, %1\n\t"
        "v_accvgpr_read_b32 %0, a114\n\tv_mul_f32 %0, %3, %0\n\tv_accvgpr_write_b32 a114, %0\n\t"
        "v_accvgpr_read_b32 %1, a115\n\tv_mul_f32 %1, %3, %1\n\tv_accvgpr_write_b32 a115, %1\n\t"
        "v_accvgpr_read_b32 %0, a116\n\tv_mul_f32 %0, %3, %0\n\tv_accvgpr_write_b32 a116, %0\n\t"
        "v_accvgpr_read_b32 %1, a117\n\tv_mul_f32 %1, %3, %1\n\tv_accvgpr_write_b32 a117, %1\n\t"
        "v_accvgpr_read_b32 %0, a118\n\tv_mul_f32 %0, %3, %0\n\tv_accvgpr_write_b32 a118, %0\n\t"
        "v_accvgpr_read_b32 %1, a119\n\tv_mul_f32 %1, %3, %1\n\tv_accvgpr_write_b32 a119, %1\n\t"
        "v_accvgpr_read_b32 %0, a120\n\tv_mul_f32 %0, %3, %0\n\tv_accvgpr_write_b32 a120, %0\n\t"
        "v_accvgpr_read_b32 %1, a121\n\tv_mul_f32 %1, %3, %1\n\tv_accvgpr_write_b32 a121, %1\n\t"
        "v_accvgpr_read_b32 %0, a122\n\tv_mul_f32 %0, %3, %0\n\tv_accvgpr_write_b32 a122, %0\n\t"
        "v_accvgpr_read_b32 %1, a123\n\tv_mul_f32 %1, %3, %1\n\tv_accvgpr_write_b32 a123, %1\n\t"
        "v_accvgpr_read_b32 %0, a124\n\tv_mul_f32 %0, %3, %0\n\tv_accvgpr_write_b32 a124, %0\n\t"
        "v_accvgpr_read_b32 %1, a125\n\tv_mul_f32 %1, %3, %1\n\tv_accvgpr_write_b32 a125, %1\n\t"
        "v_accvgpr_read_b32 %0, a126\n\tv_mul_f32 %0, %3, %0\n\tv_accvgpr_write_b32 a126, %0\n\t"
        "v_accvgpr_read_b32 %1, a127\n\tv_mul_f32 %1, %3, %1\n\tv_accvgpr_write_b32 a127, %1\n\t"
        "s_nop 1" : "=&v"(t0), "=&v"(t1) : "v"(c.fct[0]), "v"(c.fct[1]) : W4_ACLOB);
      c.lsum[0] = fmul_s(c.lsum[0], c.fct[0]); c.lsum[1] = fmul_s(c.lsum[1], c.fct[1]); c.pend = false;
    }
  }
  asm volatile("s_waitcnt lgkmcnt(0)" ::: "memory");
  GAP();
}
template <int X, bool PEEL, bool ZERO1, int DK = 1, int DV = 1, bool KR = true> DI void phaseB(W4C& c, const int t) { phaseB_<X, PEEL, ZERO1, DK, DV, KR>(c, t, make_iseq<32>{}); }

template <int... Is> DI void k_reads(W4C& c, unsigned slot, iseq<Is...>) { (k_read<Is>(c, slot), ...); }
template <int... Is> DI void tr_reads(W4C& c, unsigned vslot, iseq<Is...>) { (tr_read<Is>(c, vslot), ...); }
template <int X, int... Cs> DI void packs(W4C& c, iseq<Cs...>) { (pack1<X, ((Cs >> 3) & 1), 2 * (Cs >> 4) + ((Cs >> 2) & 1), (Cs & 3)>(c), ...); }
template <int X, int J> DI void rowsum1_padded(W4C& c) { rowsum1<X, J>(c); asm volatile("s_nop 1"); }
template <int X, int... Js> DI void rowsums(W4C& c, iseq<Js...>) { (rowsum1_padded<X, Js>(c), ...); }
template <bool Z, int... Ms> DI void pv_all(W4C& c, iseq<Ms...>) { (pv_mfma<Ms, Z>(c), ...); }
#define PKBR 0
#define PKB 8
#define WPACK (48u << 20)
#define WRING 0x10000u
#define WSLOT(kt) ((((kt) + 1) & 1) * 0xC000)
#define KC0 17
struct PC {
  bool cold;
  unsigned vwsrc, vwb, vxs[2], vxb[4], m0w, m0x, sofw, sofx;
  __amdgpu_buffer_rsrc_t srdW, srdX;
  bf16x8 xf[2][2];
  bf16x4 wlo[2][4], wup[2][4];
};
template <int J, int I> DI void ppiece(PC& p) {
  if constexpr (J < 9) {
    if constexpr (I < 8) {
      constexpr int q = I >> 1, hh = I & 1;
      asm volatile("s_mov_b32 m0, %0\n\ts_nop 0\n\tbuffer_load_dwordx4 %1, %2, %3 offen lds" :: "s"(p.m0w + (unsigned)WSLOT(J) + 16384u * hh + 1024u * q), "v"(p.vwsrc), "s"(p.srdW),
                   "s"(p.sofw + 8192u * (64u * J + 4u * (q >> 1)) + 128u * (q & 1) + 256u * hh) : "m0", "memory");
    } else {
      constexpr int q = I - 8;
      asm volatile("s_mov_b32 m0, %0\n\ts_nop 0\n\tbuffer_load_dwordx4 %1, %2, %3 offen lds" :: "s"(p.m0x + (unsigned)WSLOT(J) + 1024u * q), "v"(p.vxs[q & 1]), "s"(p.srdX),
                   "s"(p.sofx + 8u * 1152u * q + 128u * J) : "m0", "memory");
    }
  }
}
template <int J, int... Is> DI void ppieces(PC& p, iseq<Is...>) { (ppiece<J, Is>(p), ...); }
template <int S1, int I> DI void p_read(PC& p) {
  constexpr int kt = S1 >> 2, ks = S1 & 3, db = I >> 1, st = S1 & 1;
  if constexpr (I < 8) {
    constexpr int off = WSLOT(kt) + 4096 * ks + 512 * db + 2048 * (I & 1);
    if constexpr ((I & 1) == 0) asm volatile("ds_read_b64_tr_b16 %0, %1 offset:%2" : "=v"(p.wlo[st][db]) : "v"(p.vwb), "i"(off) : "memory");
    else                        asm volatile("ds_read_b64_tr_b16 %0, %1 offset:%2" : "=v"(p.wup[st][db]) : "v"(p.vwb), "i"(off) : "memory");
  } else asm volatile("ds_read_b128 %0, %1 offset:%2" : "=v"(p.xf[st][I - 8]) : "v"(p.vxb[ks]), "i"(WSLOT(kt) + 4096 * (I - 8)) : "memory");
}
template <int S, int M> DI void p_mfma(PC& p, const bf16x8 (&wf)[4]) {
  constexpr int db = M >> 1, qb = M & 1, ao = AO_(db, qb);
  if constexpr (S == 0) asm volatile("v_mfma_f32_32x32x16_bf16 a[%c2:%c3], %0, %1, 0" :: "v"(wf[db]), "v"(p.xf[S & 1][qb]), "i"(ao), "i"(ao + 15) : W4_ACLOB);
  else                  asm volatile("v_mfma_f32_32x32x16_bf16 a[%c2:%c3], %0, %1, a[%c2:%c3]" :: "v"(wf[db]), "v"(p.xf[S & 1][qb]), "i"(ao), "i"(ao + 15) : W4_ACLOB);
}
#define P_WSET(s) "+v"(p.wlo[s][0]), "+v"(p.wlo[s][1]), "+v"(p.wlo[s][2]), "+v"(p.wlo[s][3]), "+v"(p.wup[s][0]), "+v"(p.wup[s][1]), "+v"(p.wup[s][2]), "+v"(p.wup[s][3]), "+v"(p.xf[s][0]), "+v"(p.xf[s][1])
template <int T2> DI void kv_piece(W4C& c, int q) {
  if constexpr (T2 < 2) asm volatile("s_nop 4\n\ts_mov_b32 m0, %0\n\ts_nop 0\n\tbuffer_load_dwordx4 %1, %2, %3 offen" KVNT " lds"
                     :: "s"(c.m0k + SLOT(2 + T2) + 1024u * q), "v"(c.vksrc[q & 1]), "s"(c.srdK), "s"(KTILEB * (unsigned)(2 + T2) + c.sofk + KRS * 4u * q) : "m0", "memory");
  else asm volatile("s_nop 4\n\ts_mov_b32 m0, %0\n\ts_nop 0\n\tbuffer_load_dwordx4 %1, %2, %3 offen" KVNT " lds"
                     :: "s"(c.m0v + SLOT(2) + 1024u * q), "v"(c.vvsrc), "s"(c.srdV), "s"(KTILEB * 2u + c.sofv + KRS * 8u * (q >> 1) + 128u * (q & 1)) : "m0", "memory");
}
template <int T2> DI void kv0_piece(W4C& c, int q) {
  constexpr unsigned t = T2 & 1;
  if constexpr (T2 < 2) asm volatile("s_nop 4\n\ts_mov_b32 m0, %0\n\ts_nop 0\n\tbuffer_load_dwordx4 %1, %2, %3 offen" KVNT " lds"
                     :: "s"(c.m0k + SLOT(t) + 1024u * q), "v"(c.vksrc[q & 1]), "s"(c.srdK), "s"(KTILEB * t + c.sofk + KRS * 4u * q) : "m0", "memory");
  else asm volatile("s_nop 4\n\ts_mov_b32 m0, %0\n\ts_nop 0\n\tbuffer_load_dwordx4 %1, %2, %3 offen" KVNT " lds"
                     :: "s"(c.m0v + SLOT(t) + 1024u * q), "v"(c.vvsrc), "s"(c.srdV), "s"(KTILEB * t + c.sofv + KRS * 8u * (q >> 1) + 128u * (q & 1)) : "m0", "memory");
}
template <int S> DI void pstep(PC& p, W4C& c) {
  constexpr int kt = S >> 2, ks = S & 3, cur = S & 1;
  constexpr int S1 = S + 1, kt1 = S1 >> 2;
  constexpr bool rd = S1 < 36;
  if constexpr (ks == 3 && rd) {
    if constexpr (S == KC0 + 2 || S == KC0 + 6) {
      if (p.cold) asm volatile("s_waitcnt vmcnt(8)" ::: "memory"); else asm volatile("s_waitcnt vmcnt(0)" ::: "memory");
      asm volatile("s_waitcnt lgkmcnt(0)"  : P_WSET(cur) :: "memory");
    } else asm volatile("s_waitcnt vmcnt(0) lgkmcnt(0)"  : P_WSET(cur) :: "memory");
    asm volatile("s_barrier" ::: "memory");
  } else asm volatile("s_waitcnt lgkmcnt(0)" : P_WSET(cur) :: "memory");
  GAP();
  bf16x8 wf[4];
  #pragma unroll
  for (int db = 0; db < 4; ++db) wf[db] = __builtin_shufflevector(p.wlo[cur][db], p.wup[cur][db], 0, 1, 2, 3, 4, 5, 6, 7);
  asm volatile("s_nop 1" ::: "memory");
  constexpr int J = (ks == 3) ? kt + 2 : (ks == 0 && kt > 0) ? kt + 1 : 99, I0 = (ks == 3) ? 0 : 6;
  constexpr int KV = (S >= 31 && S <= 33) ? S - 31 : -1;
  p_mfma<S, 0>(p, wf); if constexpr (rd) { p_read<S1, 8>(p); p_read<S1, 9>(p); p_read<S1, 0>(p); } ppiece<J, I0 + 0>(p); if constexpr (KV >= 0) kv_piece<KV>(c, 0); GAP();
  p_mfma<S, 1>(p, wf); if constexpr (rd) { p_read<S1, 1>(p); p_read<S1, 2>(p); p_read<S1, 3>(p); } ppiece<J, I0 + 1>(p); if constexpr (KV >= 0) kv_piece<KV>(c, 1); GAP();
  p_mfma<S, 2>(p, wf); if constexpr (rd) { p_read<S1, 4>(p); p_read<S1, 5>(p); } ppiece<J, I0 + 2>(p); if constexpr (KV >= 0) kv_piece<KV>(c, 2); GAP();
  p_mfma<S, 3>(p, wf); if constexpr (rd) { p_read<S1, 6>(p); p_read<S1, 7>(p); } ppiece<J, I0 + 3>(p); if constexpr (KV >= 0) kv_piece<KV>(c, 3); GAP();
  constexpr int KC = S == KC0 ? 0 : S == KC0 + 1 ? 1 : S == KC0 + 4 ? 2 : S == KC0 + 5 ? 3 : -1;
  if constexpr (KC >= 0) { if (p.cold) { kv0_piece<KC>(c, 0); kv0_piece<KC>(c, 1); kv0_piece<KC>(c, 2); kv0_piece<KC>(c, 3); } GAP(); }
  p_mfma<S, 4>(p, wf); ppiece<J, I0 + 4>(p); GAP();
  p_mfma<S, 5>(p, wf); ppiece<J, I0 + 5>(p); GAP();
  p_mfma<S, 6>(p, wf); GAP();
  p_mfma<S, 7>(p, wf); GAP();
}
template <int... Ss> DI void psteps(PC& p, W4C& c, iseq<Ss...>) { (pstep<Ss>(p, c), ...); }
template <int... Is> DI void p_reads0(PC& p, iseq<Is...>) { (p_read<0, Is>(p), ...); }

#define STAMPRT(i) do {} while (0)
#define STAMPIT(i) do {} while (0)
struct Ptrs { const void* x[2]; const void* k[2]; const void* v[2]; const void* wq[2]; };

extern "C" __global__ __launch_bounds__(256, 1)
void attn_fused(const char* __restrict__ xA, const char* __restrict__ kA, const char* __restrict__ vA, const char* __restrict__ wqA,
                const char* __restrict__ xB, const char* __restrict__ kB, const char* __restrict__ vB, const char* __restrict__ wqB, char* __restrict__ Ows,
                const char* __restrict__ woA, const char* __restrict__ woB) {
  extern __shared__ __attribute__((aligned(16))) char lds[];
  const unsigned tid = threadIdx.x, lane = tid & 63u, wid = tid >> 6, l32 = lane & 31u, hi = lane >> 5;
  constexpr int T = NT;
  const float C = 0.12751743082f;
  W4C c;
  c.T = T; c.THR = THR;
  c.ldsb = (unsigned)(size_t)lds;
  #pragma unroll
  for (int j = 0; j < 4; ++j) c.vkb[j] = c.ldsb + KRING + 256u * l32 + 16u * ((2u * j + hi) ^ (l32 & 7u));
  c.vvb = c.ldsb + VRING + (((lane & 3u) << 3) | (((lane >> 2) & 3u) << 6) | (((lane >> 4) & 1u) << 5) | (hi << 8));
  #pragma unroll
  for (int par = 0; par < 2; ++par) c.vksrc[par] = KRS * (lane >> 4) + 16u * ((lane & 15u) ^ (4u * par + (lane >> 4)));
  c.vvsrc = KRS * ((lane >> 2) & 7u) + 64u * hi + 16u * (lane & 3u);
  c.m0k = rfl(c.ldsb + KRING + 4096u * wid); c.m0v = rfl(c.ldsb + VRING + 4096u * wid);
  c.sofk = rfl(KRS * 16u * wid); c.sofv = rfl(KRS * 16u * wid);

  const unsigned g = blockIdx.x, xcd = g & 7u;
  const unsigned kvh = 4u * (xcd & 3u) + ((g >> 3) & 3u), b = 8u * (xcd >> 2) + (g >> 5);
  const size_t kvoff = (size_t)(b * HKV + kvh) * SK * 256;
  for (int br = 0; br < 2; ++br) {
    unsigned lb;
    asm volatile("v_mbcnt_lo_u32_b32 %0, -1, 0\n\tv_mbcnt_hi_u32_b32 %0, -1, %0" : "=v"(lb));
    const unsigned l32b = lb & 31u, hib = lb >> 5;
    const char* Kbase = (br ? kB : kA) + kvoff;
    const char* Vbase = (br ? vB : vA) + kvoff;
    c.srdK = __builtin_amdgcn_make_buffer_rsrc((void*)Kbase, (short)0, SK * 256, SRD_WORD3);
    c.srdV = __builtin_amdgcn_make_buffer_rsrc((void*)Vbase, (short)0, SK * 256, SRD_WORD3);
    auto dma_k = [&](int tk) {
      const unsigned slot = SLOT(tk), skv = KTILEB * (unsigned)tk;
      #pragma unroll
      for (int q = 0; q < 4; ++q)
        asm volatile("s_nop 4\n\ts_mov_b32 m0, %0\n\ts_nop 0\n\tbuffer_load_dwordx4 %1, %2, %3 offen" KVNT " lds"
                     :: "s"(rfl(c.m0k + slot + 1024u * q)), "v"(c.vksrc[q & 1]), "s"(c.srdK), "s"(rfl(skv + c.sofk + KRS * 4u * q)) : "m0", "memory");
    };
    auto dma_v = [&](int tv) {
      const unsigned slot = SLOT(tv), skv = KTILEB * (unsigned)tv;
      #pragma unroll
      for (int q = 0; q < 4; ++q)
        asm volatile("s_nop 4\n\ts_mov_b32 m0, %0\n\ts_nop 0\n\tbuffer_load_dwordx4 %1, %2, %3 offen" KVNT " lds"
                     :: "s"(rfl(c.m0v + slot + 1024u * q)), "v"(c.vvsrc), "s"(c.srdV), "s"(rfl(skv + c.sofv + KRS * 8u * (q >> 1) + 128u * (q & 1))) : "m0", "memory");
    };
    auto l2_prefetch = [&](int live) {
      const unsigned pb = live ? 1u : 0u;
      __amdgpu_buffer_rsrc_t sw = __builtin_amdgcn_make_buffer_rsrc((void*)((pb ? wqB : wqA) + 512u * kvh), (short)0, live < 0 ? 0 : (int)(DM * 8192 - 512u * kvh), SRD_WORD3);
      __amdgpu_buffer_rsrc_t sx = __builtin_amdgcn_make_buffer_rsrc((void*)((pb ? xB : xA) + (size_t)b * SQ * 1152), (short)0, live < 0 ? 0 : SQ * 1152, SRD_WORD3);
      const unsigned i0 = 72u * wid + lb, i1 = 72u * wid + 64u + (lb & 7u);
      const unsigned w0 = ((g >> 5) * 72u + (i0 >> 2)) * 8192u + (i0 & 3u) * 128u, w1 = ((g >> 5) * 72u + (i1 >> 2)) * 8192u + (i1 & 3u) * 128u;
      const unsigned x0 = (((g >> 3) & 3u) * 288u + i0) * 128u, x1 = (((g >> 3) & 3u) * 288u + i1) * 128u;
      const unsigned md = rfl(c.ldsb + 0x20000u + 4096u * wid);
      asm volatile("s_nop 4\n\ts_mov_b32 m0, %0\n\ts_nop 0\n\tbuffer_load_dword %1, %2, 0 offen lds" :: "s"(md), "v"(w0), "s"(sw) : "m0", "memory");
      asm volatile("s_nop 4\n\tbuffer_load_dword %0, %1, 0 offen lds" :: "v"(w1), "s"(sw) : "memory");
      asm volatile("s_nop 4\n\tbuffer_load_dword %0, %1, 0 offen lds" :: "v"(x0), "s"(sx) : "memory");
      asm volatile("s_nop 4\n\tbuffer_load_dword %0, %1, 0 offen lds" :: "v"(x1), "s"(sx) : "memory");
    };
    auto wo_prefetch = [&](int live) {
      __amdgpu_buffer_rsrc_t sw = __builtin_amdgcn_make_buffer_rsrc((void*)((g < 128u ? woA : woB) + (size_t)(g & 127u) * 36864u), (short)0, live, SRD_WORD3);
      const unsigned w0 = (72u * wid + lb) * 128u, w1 = (72u * wid + 64u + (lb & 7u)) * 128u;
      const unsigned md = rfl(c.ldsb + 0x20000u + 4096u * wid);
      asm volatile("s_nop 4\n\ts_mov_b32 m0, %0\n\ts_nop 0\n\tbuffer_load_dword %1, %2, 0 offen lds" :: "s"(md), "v"(w0), "s"(sw) : "m0", "memory");
      asm volatile("s_nop 4\n\tbuffer_load_dword %0, %1, 0 offen lds" :: "v"(w1), "s"(sw) : "memory");
    };
    auto pk_issue = [&](int round) {
      __amdgpu_buffer_rsrc_t sw = __builtin_amdgcn_make_buffer_rsrc((void*)((g < 128u ? woA : woB) + (size_t)(g & 127u) * 36864u), (short)0, 36864, SRD_WORD3);
      unsigned l3;
      asm volatile("v_mbcnt_lo_u32_b32 %0, -1, 0\n\tv_mbcnt_hi_u32_b32 %0, -1, %0" : "=v"(l3));
      const unsigned md = rfl(c.ldsb + 0x20000u + 8192u * wid), so = rfl(288u * wid);
      if (round == 0) {
        const unsigned vo = (l3 >> 4) * 1152u + (l3 & 15u) * 16u;
        #pragma unroll
        for (int p = 0; p < 8; ++p)
          asm volatile("s_nop 4\n\ts_mov_b32 m0, %0\n\ts_nop 0\n\tbuffer_load_dwordx4 %1, %2, %3 offen lds" :: "s"(md + 1024u * p), "v"(vo), "s"(sw), "s"(so + 4608u * p) : "m0", "memory");
      } else {
        const unsigned vo = (l3 >> 1) * 1152u + (l3 & 1u) * 16u;
        asm volatile("s_nop 4\n\ts_mov_b32 m0, %0\n\ts_nop 0\n\tbuffer_load_dwordx4 %1, %2, %3 offen lds" :: "s"(md + 1024u), "v"(vo), "s"(sw), "s"(so + 256u) : "m0", "memory");
      }
    };
    auto pk_store = [&](int round) {
      unsigned l3;
      asm volatile("v_mbcnt_lo_u32_b32 %0, -1, 0\n\tv_mbcnt_hi_u32_b32 %0, -1, %0" : "=v"(l3));
      const unsigned c4 = l3 >> 4, pitch = round ? 32u : 256u;
      const unsigned rd = c.ldsb + 0x20000u + (round ? 1024u : 0u) + 8192u * wid + (16u * (c4 >> 1) + 4u * (c4 & 1u) + ((l3 >> 2) & 3u)) * pitch + 8u * (l3 & 3u), rd2 = rd + 8u * pitch;
      __amdgpu_buffer_rsrc_t sp = __builtin_amdgcn_make_buffer_rsrc((void*)(Ows + WPACK + (size_t)g * 36864u), (short)0, 36864, SRD_WORD3);
      const unsigned vo = l3 * 16u, so = rfl(9216u * wid);
      asm volatile("s_waitcnt vmcnt(16)\n\ts_nop 4" ::: "memory");
      #pragma unroll
      for (int t0 = 0; t0 < 8; t0 += PKB) {
        if (round && t0) break;
        bf16x4 lo[PKB], up[PKB];
        #pragma unroll
        for (int t = 0; t < PKB; ++t) {
          if (round && t) break;
          asm volatile("ds_read_b64_tr_b16 %0, %1 offset:%2" : "=v"(lo[t]) : "v"(rd), "i"((t0 + t) * 32) : "memory");
          asm volatile("ds_read_b64_tr_b16 %0, %1 offset:%2" : "=v"(up[t]) : "v"(rd2), "i"((t0 + t) * 32) : "memory");
        }
        asm volatile("s_waitcnt lgkmcnt(0)" ::: "memory");
        #pragma unroll
        for (int t = 0; t < PKB; ++t) {
          if (round && t) break;
          asm volatile("" : "+v"(lo[t]), "+v"(up[t]));
          const bf16x8 f = __builtin_shufflevector(lo[t], up[t], 0, 1, 2, 3, 4, 5, 6, 7);
          asm volatile("buffer_store_dwordx4 %0, %1, %2, %3 offen" :: "v"(f), "v"(vo), "s"(sp), "s"(so + 1024u * (round ? 8u : (unsigned)(t0 + t))) : "memory");
        }
        asm volatile("s_nop 1" ::: "memory");
      }
    };
    const bool warm = (br != 0);
    STAMPIT(0); STAMPRT(26);
    if (!warm) l2_prefetch(0);
    {
      PC p;
      const unsigned r3 = (lb >> 2) & 7u, s2 = lb & 3u;
      p.vwsrc = 8192u * (8u * (r3 >> 2) + (r3 & 3u)) + 64u * hib + 16u * s2;
      p.vwb = c.ldsb + WRING + 16384u * (wid >> 1) + (((((s2 & 1u) << 1) | (s2 >> 1)) << 3) | (((lb >> 2) & 3u) << 6) | (((lb >> 4) & 1u) << 5) | (hib << 8));
      #pragma unroll
      for (int par = 0; par < 2; ++par) p.vxs[par] = (lb >> 3) * 1152u + 16u * ((lb & 7u) ^ (4u * par + (lb >> 4)));
      #pragma unroll
      for (int ks = 0; ks < 4; ++ks) p.vxb[ks] = c.ldsb + WRING + 32768u + (64u * (wid & 1u) + l32b) * 128u + 16u * ((2u * ks + hib) ^ ((l32b >> 1) & 7u));
      p.cold = !warm;
      p.m0w = rfl(c.ldsb + WRING + 4096u * wid);
      p.m0x = rfl(c.ldsb + WRING + 32768u + 4096u * wid);
      p.sofw = rfl(8192u * 16u * wid);
      p.sofx = rfl(32u * 1152u * wid);
      p.srdW = __builtin_amdgcn_make_buffer_rsrc((void*)((br ? wqB : wqA) + 512u * kvh), (short)0, (int)(DM * 8192 - 512u * kvh), SRD_WORD3);
      p.srdX = __builtin_amdgcn_make_buffer_rsrc((void*)((br ? xB : xA) + (size_t)b * SQ * 1152), (short)0, SQ * 1152, SRD_WORD3);
      asm volatile("s_nop 4" ::: "memory");
      ppieces<0>(p, make_iseq<12>{});
      ppieces<1>(p, make_iseq<12>{});
      asm volatile("s_waitcnt vmcnt(12)\n\ts_barrier" ::: "memory");
      STAMPIT(1); STAMPRT(22);
      p_reads0(p, make_iseq<10>{});
      GAP();
      psteps(p, c, make_iseq<36>{});
      STAMPIT(2); STAMPRT(23);
      asm volatile("s_nop 7\n\ts_nop 7" ::: "memory");
      #pragma unroll
      for (int db = 0; db < 4; ++db)
        #pragma unroll
        for (int qb = 0; qb < 2; ++qb) {
          float o[16];
          W4_READ_O(db, qb, o);
          #pragma unroll
          for (int h2 = 0; h2 < 2; ++h2) {
            u32x4 w;
            #pragma unroll
            for (int e = 0; e < 4; ++e) w[e] = cvtpk(fmul_s(o[8 * h2 + 2 * e], C), fmul_s(o[8 * h2 + 2 * e + 1], C));
            q_write(qb, 2 * db + h2, w);
          }
        }
    }
    if (!warm) { _Pragma("unroll") for (int j = 0; j < 4; ++j) c.kb4[j] = vadd_vol(c.vkb[j], 0u); k_reads(c, 0u, make_iseq<16>{}); }
    asm volatile("s_waitcnt lgkmcnt(0)\n\ts_nop 1" ::: "memory");

    c.mhat[0] = c.mhat[1] = 0.f; c.lsum[0] = c.lsum[1] = 0.f; c.fct[0] = c.fct[1] = 1.f; c.pend = false;

    STAMPIT(3);
    phaseA<0, true>(c, 0);  phaseB<0, true,  false>(c, 0);
    phaseA<1, false>(c, 1); phaseB<1, false, true>(c, 1);
    if (br == PKBR) pk_issue(0);
    for (int t = 2; t < T - 4; t += 2) {
      STAMPIT(3 + t);
      phaseA<0, false>(c, t);     phaseB<0, false, false>(c, t);
      phaseA<1, false>(c, t + 1); phaseB<1, false, false>(c, t + 1);
    }
    c.srdKn = __builtin_amdgcn_make_buffer_rsrc((void*)(kB + kvoff), (short)0, br ? 0 : SK * 256, SRD_WORD3);
    c.srdVn = __builtin_amdgcn_make_buffer_rsrc((void*)(vB + kvoff), (short)0, br ? 0 : SK * 256, SRD_WORD3);
    if (br == PKBR) { pk_store(0); pk_issue(1); }
    STAMPIT(15); STAMPRT(24);
    phaseA<0, false, true, false>(c, T - 4); phaseB<0, false, false, 2, 1, true>(c, T - 4);
    phaseA<1, false, true, true >(c, T - 3); phaseB<1, false, false, 2, 2, true>(c, T - 3);
    STAMPIT(17);
    phaseA<0, false, true, true >(c, T - 2); phaseB<0, false, false, 0, 2, true>(c, T - 2);
    if (br == PKBR) pk_store(1);
    l2_prefetch(br ? -1 : 1);
    phaseA<1, false, true, true >(c, T - 1); phaseB<1, false, false, 0, 0, true>(c, T - 1);
    STAMPIT(19); STAMPRT(25);
    {
      const unsigned vslot = vadd_vol(c.vvb, SLOT(T - 1));
      tr_reads(c, vslot, make_iseq<32>{});
      #pragma unroll
      for (int e = 0; e < 32; ++e) c.S[1][1][e >> 4][e & 15] = __builtin_amdgcn_exp2f(c.S[1][1][e >> 4][e & 15]);
      packs<1>(c, make_iseq<32>{});
      rowsums<1>(c, iseq<8, 9, 10, 11, 12, 13, 14, 15, 24, 25, 26, 27, 28, 29, 30, 31>{});
      #pragma unroll
      for (int qb = 0; qb < 2; ++qb) { const float rs = fadd_s(c.rs0[qb], c.rs1[qb]);
        auto pr = __builtin_amdgcn_permlane32_swap(__float_as_uint(rs), __float_as_uint(rs), false, false);
        c.lsum[qb] = fadd_s(c.lsum[qb], fadd_s(__uint_as_float(pr[0]), __uint_as_float(pr[1]))); }
      asm volatile("s_waitcnt lgkmcnt(0)" : W4_VLO :: "memory"); asm volatile("" : W4_VUP :: "memory");
      GAP();
      #pragma unroll
      for (int ks = 0; ks < 4; ++ks)
        #pragma unroll
        for (int db = 0; db < 4; ++db) c.Vf[db][ks] = __builtin_shufflevector(c.vlo[db][ks], c.vup[db][ks], 0, 1, 2, 3, 4, 5, 6, 7);
      asm volatile("s_nop 1" ::: "memory");
      pv_all<false>(c, make_iseq<32>{});
      asm volatile("s_nop 7\n\ts_nop 7" ::: "memory");
    }
    STAMPIT(20);
    {
      char* obase = Ows + ((size_t)((br * NB + b) * 8 + 4 * (wid & 1u)) * 128 + (2u * kvh + (wid >> 1)) * 4u) * 1024;
      const unsigned loff = (l32b >> 4) * 131072u + (16u * hib + (l32b & 15u)) * 16u;
      #pragma unroll
      for (int qb = 0; qb < 2; ++qb) {
        const float il = __builtin_amdgcn_rcpf(c.lsum[qb]);
        #pragma unroll
        for (int db = 0; db < 4; ++db) {
          float o[16];
          W4_READ_O(db, qb, o);
          #pragma unroll
          for (int h2 = 0; h2 < 2; ++h2) {
            u32x4 w;
            #pragma unroll
            for (int e = 0; e < 4; ++e) w[e] = cvtpk(fmul_s(o[8 * h2 + 2 * e], il), fmul_s(o[8 * h2 + 2 * e + 1], il));
            *(u32x4*)(obase + (size_t)(2 * qb) * 131072 + db * 1024 + h2 * 512 + loff) = w;
          }
        }
      }
    }
    STAMPIT(21); STAMPRT(27);
  }
}

#define OB 1
constexpr int OSLOT = 10240;
constexpr int SHM_OPROJ = 16 * OSLOT;
struct OC {
  unsigned vsrc[5], rdb, m0b, vo, vp, junk;
  __amdgpu_buffer_rsrc_t srdW, srdO, srdP;
  unsigned sw[2], so[4], spw, spo;
  unsigned long long* stp;
  unsigned hiw;
  f32x4 acc[4][9];
  bf16x8 o[2][4];
};
DI void sadd_vol(unsigned& s, unsigned inc) { asm volatile("s_add_u32 %0, %0, %1" : "+s"(s) : "s"(inc) : "scc"); }
template <int I, int P> DI void op_dma_w1(OC& c) {
  if constexpr (I < 16 && P < 9) {
    asm volatile("s_add_u32 m0, %0, %1\n\ts_nop 0\n\tbuffer_load_dwordx4 %2, %3, %4 offen lds" :: "s"(c.m0b), "i"((I & 1) * OSLOT + 1024 * P), "v"(c.vsrc[P % 5]), "s"(c.srdW), "s"(c.sw[P / 5]) : "m0", "scc", "memory");
    if constexpr (P == 4 || P == 8) sadd_vol(c.sw[P / 5], 256u * 1152u);
  }
}
template <int I, int... Ps> DI void op_dma_w(OC& c, iseq<Ps...>) { (op_dma_w1<I, Ps>(c), ...); }
template <int I, int Q> DI void op_load_o(OC& c) {
  if constexpr (I < 16) {
    asm volatile("buffer_load_dwordx4 %0, %1, %2, %3 offen" : "=v"(c.o[I & 1][Q]) : "v"(c.vo), "s"(c.srdO), "s"(c.so[Q]) : "memory");
    sadd_vol(c.so[Q], 8192u);
  }
}
DI void op_prefetch(OC& c) {
}
#define OSTAMP(i) do {} while (0)
template <int I> DI void op_step(OC& c) {
  constexpr int s = I & 1;
  if constexpr (I < 15) asm volatile("s_waitcnt vmcnt(13)" : "+v"(c.o[s][0]), "+v"(c.o[s][1]), "+v"(c.o[s][2]), "+v"(c.o[s][3]) :: "memory");
  else                  asm volatile("s_waitcnt vmcnt(0)"  : "+v"(c.o[s][0]), "+v"(c.o[s][1]), "+v"(c.o[s][2]), "+v"(c.o[s][3]) :: "memory");
  OSTAMP(2 + 2 * I);
  bf16x8 wf[9];
  #pragma unroll
  for (int t = 0; t < 9; ++t) asm volatile("ds_read_b128 %0, %1 offset:%2" : "=v"(wf[t]) : "v"(c.rdb), "i"(s * OSLOT + t * 1024) : "memory");
  asm volatile("s_waitcnt lgkmcnt(0)" : "+v"(wf[0]), "+v"(wf[1]), "+v"(wf[2]), "+v"(wf[3]), "+v"(wf[4]), "+v"(wf[5]), "+v"(wf[6]), "+v"(wf[7]), "+v"(wf[8]) :: "memory");
  asm volatile("s_nop 1" ::: "memory");
#define OP_M(Q, t) asm volatile("v_mfma_f32_16x16x32_bf16 %0, %1, %2, %0" : "+v"(c.acc[Q][t]) : "v"(wf[t]), "v"(c.o[s][Q]));
#define OP_Q(Q) \
  OP_M(Q, 0) OP_M(Q, 1) OP_M(Q, 2) GAP(); op_dma_w1<I + 2, 3 * Q + 0>(c); GAP(); \
  OP_M(Q, 3) OP_M(Q, 4) OP_M(Q, 5) GAP(); op_dma_w1<I + 2, 3 * Q + 1>(c); GAP(); \
  OP_M(Q, 6) OP_M(Q, 7) OP_M(Q, 8) GAP(); op_dma_w1<I + 2, 3 * Q + 2>(c); op_load_o<I + 2, Q>(c); GAP();
  OP_Q(0) OP_Q(1) OP_Q(2) OP_Q(3)
  op_prefetch(c);
  OSTAMP(3 + 2 * I);
#undef OP_M
#undef OP_Q
}
template <int... Is> DI void op_steps(OC& c, iseq<Is...>) { (op_step<Is>(c), ...); }

extern "C" __global__ __launch_bounds__(512, 1)
void oproj(const char* __restrict__ Ows, const char* __restrict__ woA, const char* __restrict__ woB, unsigned short* __restrict__ out) {
  extern __shared__ __attribute__((aligned(16))) char lds[];
  const unsigned tid = threadIdx.x, lane = tid & 63u, wid = __builtin_amdgcn_readfirstlane(tid >> 6);
  const unsigned g = blockIdx.x, xcd = g & 7u, br = xcd >> 2, nq = (g >> 3) & 3u, rb = (xcd & 3u) * 8u + (g >> 5);
  const unsigned ldsb = (unsigned)(size_t)lds;
  OC c;
  c.stp = (unsigned long long*)(Ows + (72u << 20)) + (blockIdx.x * 8 + wid) * 64;
  OSTAMP(0);
  const char* wo = Ows + WPACK + br * (4096u * 1152u) + nq * 9216u;
  c.srdW = __builtin_amdgcn_make_buffer_rsrc((void*)wo, (short)0, (int)(4096 * 1152 - nq * 9216u), SRD_WORD3);
  const char* ob = Ows + (size_t)((br * 32u + rb) * 4u) * 131072;
  c.srdO = __builtin_amdgcn_make_buffer_rsrc((void*)ob, (short)0, 4 * 131072, SRD_WORD3);
  #pragma unroll
  for (int p = 0; p < 5; ++p) c.vsrc[p] = lane * 16u + 1024u * p;
  c.vo = lane * 16u;
  c.hiw = wid >> 2;
  c.sw[0] = wid * 32u * 1152u; c.sw[1] = c.sw[0] + 5120u;
  #pragma unroll
  for (int q = 0; q < 4; ++q) c.so[q] = wid * 1024u + q * 131072u;
  c.srdP = __builtin_amdgcn_make_buffer_rsrc((void*)(br ? woB : woA), (short)0, 4096 * 1152, SRD_WORD3);
  c.vp = lane < 9u ? lane * 128u : 0x7f000000u;
  c.spw = 2u * 256u * 1152u + ((g >> 3) * 8u + wid) * 9u * 128u; c.spo = 2u * 8192u + nq * 131072u + wid * 1024u; c.junk = 0u;
  c.m0b = ldsb + wid * 2u * OSLOT;
  c.rdb = c.m0b + lane * 16u;
  #pragma unroll
  for (int q = 0; q < 4; ++q)
    #pragma unroll
    for (int t = 0; t < 9; ++t) c.acc[q][t] = f32x4{0.f, 0.f, 0.f, 0.f};
  asm volatile("s_nop 4" ::: "memory");
  GAP();
  op_dma_w<0>(c, make_iseq<9>{}); op_load_o<0, 0>(c); op_load_o<0, 1>(c); op_load_o<0, 2>(c); op_load_o<0, 3>(c);
  asm volatile("s_barrier" ::: "memory");
  op_dma_w<1>(c, make_iseq<9>{}); op_load_o<1, 0>(c); op_load_o<1, 1>(c); op_load_o<1, 2>(c); op_load_o<1, 3>(c);
  op_prefetch(c); op_prefetch(c);
  op_steps(c, make_iseq<16>{});
  asm volatile("s_nop 7\n\ts_nop 7" :: "v"(c.junk) : "memory");
  OSTAMP(34);
  f32x4* red = (f32x4*)lds;
  unsigned ln;
  asm volatile("v_mbcnt_lo_u32_b32 %0, -1, 0\n\tv_mbcnt_hi_u32_b32 %0, -1, %0" : "=v"(ln));
  __syncthreads();
  OSTAMP(36);
  {
    const bool upw = wid >= 4;
    f32x4* wr = red + wid * 18 * 64 + ln;
    const f32x4* rd = red + (wid ^ 4u) * 18 * 64 + ln;
    #pragma unroll
    for (int q = 0; q < 2; ++q)
      #pragma unroll
      for (int t = 0; t < 9; ++t) wr[(q * 9 + t) * 64] = upw ? c.acc[q][t] : c.acc[q + 2][t];
    __syncthreads();
    #pragma unroll
    for (int q = 0; q < 2; ++q)
      #pragma unroll
      for (int t = 0; t < 9; ++t) c.acc[q][t] = (upw ? c.acc[q + 2][t] : c.acc[q][t]) + rd[(q * 9 + t) * 64];
    __syncthreads();
  }
  OSTAMP(37);
  {
    const bool upw = (wid & 2u) != 0;
    f32x4* wr = red + wid * 9 * 64 + ln;
    const f32x4* rd = red + (wid ^ 2u) * 9 * 64 + ln;
    #pragma unroll
    for (int t = 0; t < 9; ++t) wr[t * 64] = upw ? c.acc[0][t] : c.acc[1][t];
    __syncthreads();
    #pragma unroll
    for (int t = 0; t < 9; ++t) c.acc[0][t] = (upw ? c.acc[1][t] : c.acc[0][t]) + rd[t * 64];
    __syncthreads();
  }
  OSTAMP(38);
  {
    const bool odd = (wid & 1u) != 0;
    f32x4* wr = red + wid * 5 * 64 + ln;
    const f32x4* rd = red + (wid ^ 1u) * 5 * 64 + ln;
    #pragma unroll
    for (int t = 0; t < 5; ++t) { if (t < 4) wr[t * 64] = odd ? c.acc[0][t] : c.acc[0][t + 5]; else if (odd) wr[t * 64] = c.acc[0][4]; }
    __syncthreads();
    const unsigned Q = 2u * (wid >> 2) + ((wid >> 1) & 1u);
    unsigned short* orow = out + (size_t)((rb * 64u + Q * 16u + (ln & 15u))) * 1152 + br * 576u + nq * 144u + 4u * (ln >> 4) + (odd ? 80u : 0u);
    #pragma unroll
    for (int t = 0; t < 5; ++t) {
      if (t < 4 || !odd) {
        const f32x4 o2 = (odd ? c.acc[0][t < 4 ? t + 5 : 8] : c.acc[0][t]) + rd[t * 64];
        u32x2 w; w[0] = cvtpk(o2[0], o2[1]); w[1] = cvtpk(o2[2], o2[3]);
        *(u32x2*)(orow + 16 * t) = w;
      }
    }
  }
  OSTAMP(35);
}

extern "C" void kernel_launch(void* const* d_in, const int* in_sizes, int n_in, void* d_out, int out_size, void* d_ws, size_t ws_size, hipStream_t stream) {
  (void)in_sizes; (void)n_in; (void)out_size; (void)ws_size;
  hipFuncSetAttribute((const void*)attn_fused, hipFuncAttributeMaxDynamicSharedMemorySize, (int)SHM_ATTN);
  hipFuncSetAttribute((const void*)oproj, hipFuncAttributeMaxDynamicSharedMemorySize, SHM_OPROJ);
  hipLaunchKernelGGL(attn_fused, dim3(256), dim3(256), SHM_ATTN, stream,
                     (const char*)d_in[0], (const char*)d_in[1], (const char*)d_in[2], (const char*)d_in[3],
                     (const char*)d_in[5], (const char*)d_in[6], (const char*)d_in[7], (const char*)d_in[8], (char*)d_ws, (const char*)d_in[4], (const char*)d_in[9]);
  hipLaunchKernelGGL(oproj, dim3(256), dim3(512), SHM_OPROJ, stream, (const char*)d_ws, (const char*)d_in[4], (const char*)d_in[9], (unsigned short*)d_out);
}
```
